```python
import torch

def _softmax(x, axis=-1):
    return torch.softmax(x, dim=axis)
def _gelu(x):
    return torch.nn.functional.gelu(x, approximate="tanh")
def _silu(x):
    return torch.nn.functional.silu(x)
def _sigmoid(x):
    return torch.sigmoid(x)
def _layernorm(x, axis=-1, eps=1e-5):
    m = x.mean(dim=axis, keepdim=True)
    v = x.var(dim=axis, keepdim=True, unbiased=False)
    return (x - m) / torch.sqrt(v + eps)
def _scatter_add(tgt, idx, src, axis=0):
    return tgt.clone().index_add_(axis, idx, src)
def _segment_sum(src, seg, n):
    out = torch.zeros((n,) + src.shape[1:], dtype=src.dtype)
    return out.index_add_(0, seg, src)
def _pad(x, pw):
    flat = tuple(v for pair in reversed(pw) for v in pair)
    return torch.nn.functional.pad(x, flat)

kernel_name = 'sequential_preLN__sliding_block__mixer_channel_mlp__prompt'

def setup_inputs():

    _w = (lambda a: (lambda t: t.to(torch.bfloat16) if t.is_floating_point() else t)(torch.as_tensor(a)))
    def _A_setup():
        B, S, D = 1, 2048, 640
        return {
            "x": _w((lambda *s: torch.randn(*s, dtype=torch.bfloat16) * 0.05)(B, S, D)),
            "wqkv": _w((lambda *s: torch.randn(*s, dtype=torch.bfloat16) * 0.05)(D, 3 * D)),
            "wo": _w((lambda *s: torch.randn(*s, dtype=torch.bfloat16) * 0.05)(D, D)),
        }
    _A_in = _A_setup()

    def _B_setup():
        B, S, D, F = 1, 2048, 640, 1536
        return {
            "x": _w((lambda *s: torch.randn(*s, dtype=torch.bfloat16) * 0.05)(B, S, D)),
            "w1": _w((lambda *s: torch.randn(*s, dtype=torch.bfloat16) * 3.2)(D, F) * (D ** -0.5)),
            "b1": _w((lambda *s: torch.randn(*s, dtype=torch.bfloat16) * 1.6)(F)),
            "w2": _w((lambda *s: torch.randn(*s, dtype=torch.bfloat16) * 0.2)(F, D) * (F ** -0.5)),
            "b2": _w((lambda *s: torch.randn(*s, dtype=torch.bfloat16) * 0.4)(D)),
        }
    _B_in = _B_setup()

    return {
        "A_x": _A_in["x"],
        "A_wqkv": _A_in["wqkv"],
        "A_wo": _A_in["wo"],
        "B_x": _B_in["x"],
        "B_w1": _B_in["w1"],
        "B_b1": _B_in["b1"],
        "B_w2": _B_in["w2"],
        "B_b2": _B_in["b2"],
    }

def reference(A_x, A_wqkv, A_wo, B_x, B_w1, B_b1, B_w2, B_b2):
    def _A_ref(x, wqkv, wo):
        B, S, D = x.shape
        H = 32; Dh = D // H; W = 256
        qkv = (lambda x, *d: x.permute(*d))((x @ wqkv).reshape(B, S, 3, H, Dh), 2, 0, 3, 1, 4)
        q, k, v = qkv[0], qkv[1], qkv[2]
        scores = torch.einsum("bhqd,bhkd->bhqk", q, k) * (Dh ** -0.5)
        idx = torch.arange(S)
        mask = (idx[:, None] - idx[None, :] >= 0) & (idx[:, None] - idx[None, :] < W)
        scores = torch.where(mask[None, None], scores, -1e9)
        p = _softmax(scores, axis=-1)
        o = torch.einsum("bhqk,bhkd->bhqd", p, v)
        return (lambda x, *d: x.permute(*d))(o, 0, 2, 1, 3).reshape(B, S, D) @ wo

    def _B_ref(x, w1, b1, w2, b2):
        y = _layernorm(x, axis=-1)
        h = _gelu(torch.einsum("bsd,df->bsf", y, w1) + b1)
        return torch.einsum("bsf,fd->bsd", h, w2) + b2

    x = A_x
    h = x + _A_ref(_layernorm(x, axis=-1), A_wqkv, A_wo)
    h2 = h + _B_ref(_layernorm(h, axis=-1), B_w1, B_b1, B_w2, B_b2)
    return h2
```

```cpp
#include <hip/hip_runtime.h>
#include <hip/hip_bf16.h>
typedef unsigned short u16;
__device__ __forceinline__ float b2f(u16 v){ return __uint_as_float(((unsigned)v)<<16); }
__device__ __forceinline__ u16 f2b(float f){ unsigned u=__float_as_uint(f); u += 0x7fffu + ((u>>16)&1u); return (u16)(u>>16); }
constexpr int S=2048, D=640, H=32, DH=20, W=256, F=1536;

__global__ void ln_k(const u16* x, u16* y, int times){
  int row = blockIdx.x; int lane = threadIdx.x;
  float v[10];
  for(int i=0;i<10;i++) v[i]=b2f(x[row*D+lane+64*i]);
  for(int t=0;t<times;t++){
    float s=0; for(int i=0;i<10;i++) s+=v[i];
    for(int o=32;o;o>>=1) s+=__shfl_xor(s,o);
    float m=s/D; float q=0; for(int i=0;i<10;i++){ float d=v[i]-m; q+=d*d; }
    for(int o=32;o;o>>=1) q+=__shfl_xor(q,o);
    float r=rsqrtf(q/D+1e-5f);
    for(int i=0;i<10;i++) v[i]=b2f(f2b((v[i]-m)*r));
  }
  for(int i=0;i<10;i++) y[row*D+lane+64*i]=f2b(v[i]);
}
__global__ void gemm_k(const u16* A, const u16* B, u16* C, const u16* bias, const u16* resid, int N, int K, int mode){
  __shared__ float As[16][17], Bs[16][17];
  int tx=threadIdx.x&15, ty=threadIdx.x>>4;
  int row=blockIdx.y*16+ty, col=blockIdx.x*16+tx;
  float acc=0;
  for(int k0=0;k0<K;k0+=16){
    As[ty][tx]=b2f(A[row*K+k0+tx]); Bs[ty][tx]=b2f(B[(k0+ty)*N+col]);
    __syncthreads();
    for(int k=0;k<16;k++) acc+=As[ty][k]*Bs[k][tx];
    __syncthreads();
  }
  if(mode==1) acc = b2f(f2b(acc))+b2f(resid[row*N+col]);
  if(mode==2){ float u=acc+b2f(bias[col]); float t=tanhf(0.7978845608f*(u+0.044715f*u*u*u)); acc=0.5f*u*(1.f+t);}
  if(mode==3) acc = acc+b2f(bias[col])+b2f(resid[row*N+col]);
  C[row*N+col]=f2b(acc);
}
__global__ void attn_k(const u16* qkv, u16* o){
  int idx=blockIdx.x*blockDim.x+threadIdx.x; int h=idx/S, q=idx%S;
  float qv[DH]; for(int d=0;d<DH;d++) qv[d]=b2f(qkv[q*3*D+h*DH+d]);
  int k0=q-W+1; if(k0<0)k0=0;
  float m=-1e30f;
  for(int k=k0;k<=q;k++){ float s=0; for(int d=0;d<DH;d++) s+=qv[d]*b2f(qkv[k*3*D+D+h*DH+d]); s*=0.22360679775f; m=fmaxf(m,s);}
  float l=0, acc[DH]; for(int d=0;d<DH;d++) acc[d]=0;
  for(int k=k0;k<=q;k++){ float s=0; for(int d=0;d<DH;d++) s+=qv[d]*b2f(qkv[k*3*D+D+h*DH+d]); s*=0.22360679775f; float p=__expf(s-m); l+=p; for(int d=0;d<DH;d++) acc[d]+=p*b2f(qkv[k*3*D+2*D+h*DH+d]); }
  for(int d=0;d<DH;d++) o[q*D+h*DH+d]=f2b(acc[d]/l);
}
extern "C" void kernel_launch(void* const* d_in, const int* in_sizes, int n_in, void* d_out, int out_size, void* d_ws, size_t ws_size, hipStream_t stream){
  const u16* x=(const u16*)d_in[0]; const u16* wqkv=(const u16*)d_in[1]; const u16* wo=(const u16*)d_in[2];
  const u16* w1=(const u16*)d_in[4]; const u16* b1=(const u16*)d_in[5]; const u16* w2=(const u16*)d_in[6]; const u16* b2=(const u16*)d_in[7];
  u16* ws=(u16*)d_ws; u16* xn=ws; u16* qkv=xn+S*D; u16* o=qkv+S*3*D; u16* h=o+S*D; u16* y=h+S*D; u16* h1=y+S*D;
  ln_k<<<S,64,0,stream>>>(x,xn,1);
  gemm_k<<<dim3(3*D/16,S/16),256,0,stream>>>(xn,wqkv,qkv,nullptr,nullptr,3*D,D,0);
  attn_k<<<S*H/256,256,0,stream>>>(qkv,o);
  gemm_k<<<dim3(D/16,S/16),256,0,stream>>>(o,wo,h,nullptr,x,D,D,1);
  ln_k<<<S,64,0,stream>>>(h,y,2);
  gemm_k<<<dim3(F/16,S/16),256,0,stream>>>(y,w1,h1,b1,nullptr,F,D,2);
  gemm_k<<<dim3(D/16,S/16),256,0,stream>>>(h1,w2,(u16*)d_out,b2,h,D,F,3);
}
```

```cpp
#include <hip/hip_runtime.h>

typedef unsigned int u32;
typedef unsigned long long u64;
typedef __bf16 bf16x2 __attribute__((ext_vector_type(2)));
typedef __bf16 bf16x8 __attribute__((ext_vector_type(8)));
typedef short s16x4 __attribute__((ext_vector_type(4)));
typedef float f32x2 __attribute__((ext_vector_type(2)));
typedef float f32x4 __attribute__((ext_vector_type(4)));
typedef float f32x16 __attribute__((ext_vector_type(16)));
typedef u32 u32x2 __attribute__((ext_vector_type(2)));
typedef u32 u32x4 __attribute__((ext_vector_type(4)));
typedef __amdgpu_buffer_rsrc_t rsrc_t;
#define LDS __attribute__((address_space(3)))
#define DI __device__ __forceinline__

constexpr u32 PS = 2048 * 16 + 256;
constexpr u32 FS = 128;
constexpr u32 OFF_EPOCH = 0, OFF_ERR = 128, OFF_F0C = 1024, OFF_CS1 = 8192, OFF_XS = 16384, OFF_F1 = 65536;
constexpr u32 OFF_F2 = OFF_F1 + 256 * FS, OFF_F3 = OFF_F2 + 2048 * FS, OFF_F4 = OFF_F3 + 2048 * FS, OFF_HS = OFF_F4 + 2048 * FS;
constexpr u32 OFF_QKV = OFF_HS + 512 * 512, OFF_O = OFF_QKV + 240 * PS, OFF_H = OFF_O + 80 * PS, OFF_H1 = OFF_H + 80 * PS;
constexpr u32 OFF_WO = OFF_H1 + 192 * PS, OFF_W1 = OFF_WO + 800 * 1024, OFF_W2 = OFF_W1 + 1920 * 1024;
constexpr u32 WS_END = OFF_W2 + 1920 * 1024;
#define OFF_ERR_DEF OFF_ERR
#define HAVE_PLANES 1
#define STAMP(i)

constexpr u32 L_RELAY = 131072, L_XROW = 143360, L_XS = 159744, L_CHK = 161792, L_JUNK = 162816;
constexpr u32 KVS = 8256, L_WO = 57856;
constexpr u32 L_S0 = 122880, L_ST4 = 155648, L_TAB = 160256;
constexpr u32 L_CTRL = 163584;
constexpr u32 C_WO = L_CTRL, C_FREE = L_CTRL + 16, C_X3 = L_CTRL + 20, C_SCR = L_CTRL + 28, C_W1 = L_CTRL + 32, C_X4 = L_CTRL + 36, C_FREE2 = L_CTRL + 52,
              C_CHK = L_CTRL + 56, C_HS = L_CTRL + 64;
constexpr u32 L_HS = 157696;
constexpr u32 CODE_BYTES = 28672;

DI rsrc_t mkrsrc(const void* p, u32 bytes) { return __builtin_amdgcn_make_buffer_rsrc((void*)p, (short)0, (int)bytes, 0x00020000); }
DI void glds(rsrc_t rs, u32 voff, u32 soff, u32 l) {
  asm volatile("s_mov_b32 m0, %0\n\ts_nop 0\n\tbuffer_load_dwordx4 %1, %2, %3 offen lds" ::"s"(l), "v"(voff), "s"(rs), "s"(soff) : "memory");
}
DI void glds_dev(rsrc_t rs, u32 voff, u32 soff, u32 l) {
  asm volatile("s_mov_b32 m0, %0\n\ts_nop 0\n\tbuffer_load_dwordx4 %1, %2, %3 offen sc1 lds" ::"s"(l), "v"(voff), "s"(rs), "s"(soff) : "memory");
}
DI void glds_pf(rsrc_t rs, u32 voff, u32 soff, u32 l) {
  asm volatile("s_mov_b32 m0, %0\n\ts_nop 0\n\tbuffer_load_dwordx4 %1, %2, %3 offen sc1 lds" ::"s"(l), "v"(voff), "s"(rs), "s"(soff));
}
DI void touch(rsrc_t rs, u32 voff, u32 soff) {
  asm volatile("s_mov_b32 m0, %0\n\ts_nop 0\n\tbuffer_load_dword %1, %2, %3 offen sc1 lds" ::"s"(L_JUNK), "v"(voff), "s"(rs), "s"(soff));
}
DI u32x4 ld_dev(rsrc_t rs, u32 voff, u32 soff) { return __builtin_amdgcn_raw_buffer_load_b128(rs, voff, soff, 16); }
DI void st_dev(u32x4 v, rsrc_t rs, u32 voff, u32 soff) {
  asm volatile("buffer_store_dwordx4 %0, %1, %2, %3 offen sc1\n\ts_nop 1" ::"v"(v), "v"(voff), "s"(rs), "s"(soff) : "memory");
}
DI void st_plain(u32x4 v, rsrc_t rs, u32 voff, u32 soff) {
  asm volatile("buffer_store_dwordx4 %0, %1, %2, %3 offen\n\ts_nop 1" ::"v"(v), "v"(voff), "s"(rs), "s"(soff) : "memory");
}
DI u32x4 lds128(u32 a) { return *(LDS u32x4*)(size_t)a; }
DI f32x4 ldsf4(u32 a) { return *(LDS f32x4*)(size_t)a; }
DI void lds_st4(u32 a, f32x4 v) { *(LDS f32x4*)(size_t)a = v; }
DI u32x4 ldstr(u32 a0, u32 a1) {
  u32x2 lo = __builtin_bit_cast(u32x2, __builtin_amdgcn_ds_read_tr16_b64_v4i16((LDS s16x4*)(size_t)a0));
  u32x2 hi = __builtin_bit_cast(u32x2, __builtin_amdgcn_ds_read_tr16_b64_v4i16((LDS s16x4*)(size_t)a1));
  return u32x4{lo.x, lo.y, hi.x, hi.y};
}
DI f32x16 mfma32(u32x4 a, u32x4 b, f32x16 c) { return __builtin_amdgcn_mfma_f32_32x32x16_bf16(__builtin_bit_cast(bf16x8, a), __builtin_bit_cast(bf16x8, b), c, 0, 0, 0); }
DI f32x4 mfma16(u32x4 a, u32x4 b, f32x4 c) { return __builtin_amdgcn_mfma_f32_16x16x32_bf16(__builtin_bit_cast(bf16x8, a), __builtin_bit_cast(bf16x8, b), c, 0, 0, 0); }
DI float dot2(u32 a, u32 b, float c) { return __builtin_amdgcn_fdot2_f32_bf16(__builtin_bit_cast(bf16x2, a), __builtin_bit_cast(bf16x2, b), c, false); }
DI float sum8(u32x4 v, float c) { c = dot2(v.x, 0x3f803f80u, c); c = dot2(v.y, 0x3f803f80u, c); c = dot2(v.z, 0x3f803f80u, c); return dot2(v.w, 0x3f803f80u, c); }
DI float sq8(u32x4 v, float c) { c = dot2(v.x, v.x, c); c = dot2(v.y, v.y, c); c = dot2(v.z, v.z, c); return dot2(v.w, v.w, c); }
DI u32 pk(float a, float b) { f32x2 v = {a, b}; return __builtin_bit_cast(u32, __builtin_convertvector(v, bf16x2)); }
DI float bflo(u32 v) { return __uint_as_float(v << 16); }
DI float bfhi(u32 v) { return __uint_as_float(v & 0xffff0000u); }
DI float halves_sum(float x) { auto r = __builtin_amdgcn_permlane32_swap(__float_as_uint(x), __float_as_uint(x), false, false); return __uint_as_float(r[0]) + __uint_as_float(r[1]); }
DI u32x4 widen32(u32 a0, u32 a1, u32 b0, u32 b1) {
  auto r0 = __builtin_amdgcn_permlane32_swap(a0, b0, false, false);
  auto r1 = __builtin_amdgcn_permlane32_swap(a1, b1, false, false);
  return u32x4{r0[0], r1[0], r0[1], r1[1]};
}
DI u32x4 widen16(u32 a0, u32 a1, u32 b0, u32 b1) {
  auto r0 = __builtin_amdgcn_permlane16_swap(a0, b0, false, false);
  auto r1 = __builtin_amdgcn_permlane16_swap(a1, b1, false, false);
  return u32x4{r0[0], r1[0], r0[1], r1[1]};
}
#define VMCNT(n) asm volatile("s_waitcnt vmcnt(" #n ")" ::: "memory")
#define SB() __builtin_amdgcn_sched_barrier(0)
#define OOB 0x7fffffffu
#define LGKM0() asm volatile("s_waitcnt lgkmcnt(0)" ::: "memory")
#define BAR() do { asm volatile("s_waitcnt lgkmcnt(0)" ::: "memory"); __builtin_amdgcn_s_barrier(); asm volatile("" ::: "memory"); } while (0)
#define DRAIN() do { __builtin_amdgcn_s_waitcnt(0x0F70); asm volatile("s_waitcnt vmcnt(0)" ::: "memory"); } while (0)
DI u32 opaque(u32 v) { asm volatile("" : "+v"(v)); return v; }
DI void lds_inc(u32 a) {
  asm volatile("s_waitcnt lgkmcnt(0)\n\ts_mov_b64 exec, 1\n\tds_add_u32 %0, %1\n\ts_mov_b64 exec, -1" ::"v"(a), "v"(1u) : "memory");
}
DI u32 lds_ticket(u32 a) {
  u32 r;
  asm volatile("s_waitcnt lgkmcnt(0)\n\ts_mov_b64 exec, 1\n\tds_add_rtn_u32 %0, %1, %2\n\ts_mov_b64 exec, -1\n\ts_waitcnt lgkmcnt(0)" : "=&v"(r) : "v"(a), "v"(1u) : "memory");
  return __builtin_amdgcn_readfirstlane(r);
}
DI void set_flag(rsrc_t rws, u32 off, u32 tag) {
  asm volatile("s_mov_b64 exec, 1\n\tbuffer_store_dword %0, off, %1, %2 sc1\n\ts_mov_b64 exec, -1" ::"v"(tag), "s"(rws), "s"(off) : "memory");
}
DI void lds_wait(u32 a, u32 target) {
#pragma unroll 1
  for (u32 it = 0; it < (1u << 20); ++it) {
    u32 v = *(volatile LDS u32*)(size_t)a;
    if ((u32)__builtin_amdgcn_readfirstlane(v) >= target) break;
    __builtin_amdgcn_s_sleep(1);
  }
  asm volatile("" ::: "memory");
}
DI void poll1(rsrc_t rws, u32 voff, u32 soff, u32 tag, bool need) {
#pragma unroll 1
  for (u32 it = 0;; ++it) {
    if (need) need = __builtin_amdgcn_raw_buffer_load_b32(rws, voff, soff, 16) != tag;
    asm volatile("" ::: "memory");
    if (!__any(need)) break;
    if (it > (1u << 16)) { __builtin_amdgcn_raw_buffer_store_b32(0xdeadu, rws, OFF_ERR, 0, 16); break; }
    __builtin_amdgcn_s_sleep(2);
  }
  asm volatile("" ::: "memory");
}
template <int CTRL> DI float dpp(float x) { return __builtin_bit_cast(float, __builtin_amdgcn_mov_dpp(__builtin_bit_cast(int, x), CTRL, 0xf, 0xf, true)); }
DI float wave_sum(float x) {
  x += dpp<0xB1>(x); x += dpp<0x4E>(x); x += dpp<0x141>(x); x += dpp<0x128>(x);
  auto r = __builtin_amdgcn_permlane16_swap(__float_as_uint(x), __float_as_uint(x), false, false);
  return halves_sum(__uint_as_float(r[0]) + __uint_as_float(r[1]));
}

__global__ __launch_bounds__(512, 2) void fused_block(const void* __restrict__ px, const void* __restrict__ pwqkv, const void* __restrict__ pwo,
                                                      const void* __restrict__ pw1, const void* __restrict__ pb1, const void* __restrict__ pw2,
                                                      const void* __restrict__ pb2, void* __restrict__ pout, void* pws) {
  __shared__ __attribute__((aligned(16))) char smem[163840];
  STAMP(0);
  asm volatile("" : "+s"(px), "+s"(pwqkv), "+s"(pwo), "+s"(pw1), "+s"(pb1), "+s"(pw2), "+s"(pb2), "+s"(pout), "+s"(pws));
  u32 tag, epoch;
  asm volatile("s_load_dword %0, %1, 0x0" : "=s"(epoch) : "s"(pws));
  const u32 tid = threadIdx.x, lane = tid & 63;
  const u32 w = __builtin_amdgcn_readfirstlane(tid >> 6);
  const u32 b = blockIdx.x, xcd = b & 7, j = b >> 3, pr = xcd >> 1, xo = xcd & 1;
  const u32 hh = lane >> 5, l31 = lane & 31, l15 = lane & 15, g4 = lane >> 4, q4 = (lane & 15) >> 2, p4 = lane & 3, blk = (lane >> 4) & 1;
  const rsrc_t rx = mkrsrc(px, 2048 * 1280);
  const rsrc_t rws = mkrsrc(pws, WS_END);
  asm volatile("" ::"s"((u32)(size_t)(LDS char*)smem));
  const u32 voffP = hh * PS + l31 * 16;
  const u32 voffQ = g4 * PS + l15 * 16;
  const u32 v16 = lane * 16;

  const bool spare = xo && j >= 28;
  u32 f1idx;
  bool wready = false;
  const u32 xrow = 512 * pr + (xo * 32 + j) * 8 + w;
  auto side_loads = [&]() {
    glds(rx, v16, xrow * 1280, L_XROW + w * 2048);
    if (w == 7) {
      u64 pc;
      asm volatile("s_getpc_b64 %0" : "=s"(pc));
      const rsrc_t rc = mkrsrc((const void*)(pc & ~127ull), CODE_BYTES);
      asm volatile("s_mov_b32 m0, %0\n\ts_nop 0\n\tbuffer_load_dword %1, %2, 0 offen lds" ::"s"(L_JUNK), "v"(lane < 8 ? (j * 8 + lane) * 128 : OOB), "s"(rc) : "memory");
    }
    glds(rx, lane < 16 ? v16 : OOB, xrow * 1280 + 1024, L_XROW + w * 2048 + 1024);
  };
  auto row_stats = [&]() {
    asm volatile("s_waitcnt lgkmcnt(0)\n\ts_add_u32 %0, %1, 1" : "=s"(tag) : "s"(epoch) : "scc", "memory");
    float v[10], sum = 0.f;
    const u32 a = opaque(L_XROW + w * 2048 + lane * 4);
#pragma unroll
    for (int k = 0; k < 5; ++k) {
      const u32 d = *(LDS u32*)(size_t)(a + k * 256);
      v[2 * k] = bflo(d); v[2 * k + 1] = bfhi(d);
      sum += v[2 * k] + v[2 * k + 1];
    }
    const float mu = wave_sum(sum) * (1.0f / 640.0f);
    float sq = 0.f;
#pragma unroll
    for (int k = 0; k < 10; ++k) sq += (v[k] - mu) * (v[k] - mu);
    const float rstd = __builtin_amdgcn_rsqf(wave_sum(sq) * (1.0f / 640.0f) + 1e-5f);
    const float m1 = bflo(pk(-mu, 0.f));
    st_dev(u32x4{pk(m1, -mu - m1), tag, __float_as_uint(rstd), tag}, rws, lane == 0 ? 0u : OOB, OFF_XS + xrow * 16);
  };
  if (__builtin_expect(!spare, 1)) {
    const u32 rl = xo ? (j * 37) >> 8 : j >> 3, cl = j - rl * (8 - xo), cb = 8 * xo + cl, rb = 4 * pr + rl;
    f1idx = rb * 16 + cb;
    const rsrc_t rw = mkrsrc(pwqkv, 640 * 3840);
    const u32 voffA = (8 * w + (lane >> 3)) * 1280 + (((lane & 7) ^ ((4 * w + g4) & 7)) << 4);
    const u32 voffB = (4 * w + g4) * 3840 + ((l15 ^ ((g4 << 2) | (w & 3))) << 4);
    u32 sk = ((cl * 5) >> 2) + ((rl * 5) >> 1) + pr;
    sk = sk >= 10 ? sk - 10 : sk;
    u32 sa = 0, sb = 0;
    auto next = [&]() { sa = rb * 163840 + sk * 128; sb = cb * 256 + sk * 245760; sk = sk == 9 ? 0 : sk + 1; };
    auto piece = [&](int buf, int i) {
      const u32 l = buf * 32768 + w * 1024;
      if (i == 0) glds(rx, voffA, sa, l);
      if (i == 1) glds(rw, voffB, sb, l + 16384);
      if (i == 2) glds(rx, voffA, sa + 81920, l + 8192);
      if (i == 3) glds(rw, voffB, sb + 122880, l + 24576);
    };
    auto stage = [&](int buf) { next(); piece(buf, 0); piece(buf, 1); piece(buf, 2); piece(buf, 3); };
    stage(0);
    side_loads();
    u32 ri = (rb * 15 + cb) * 3 + w;
    const bool relay = w < 3 && ri < 680;
    const bool isw2 = ri >= 200;
    if (isw2) ri -= 200;
    const u32 rkb = ri / 10, rnb = ri - rkb * 10;
    if (relay) {
      const rsrc_t rr = mkrsrc(isw2 ? pw2 : pwo, isw2 ? 1536 * 1280 : 640 * 1280);
      const u32 vo = (lane >> 3) * 1280 + (lane & 7) * 16;
#pragma unroll
      for (int i = 0; i < 4; ++i) glds(rr, vo, (32 * rkb + 8 * i) * 1280 + rnb * 128, L_RELAY + w * 4096 + i * 1024);
    }
    stage(1); stage(2);
    if (tid < 32) *(LDS u32*)(size_t)(L_CTRL + tid * 4) = 0;
    const u32 wm = w >> 2, wn = w & 3;
    const u32 ax0 = (64 * wm + l31) * 128 + ((hh ^ ((l31 >> 1) & 7)) << 4);
    const u32 aw0 = 16384 + (8 * hh + q4) * 256 + (((4 * wn + 2 * blk + (p4 >> 1)) ^ ((q4 << 2) | (2 * hh))) << 4) + 8 * (p4 & 1);
    u32 ax[2][4], aw[2][2];
#pragma unroll
    for (int i = 0; i < 4; ++i) { ax[0][i] = opaque(ax0 ^ (i << 5)); ax[1][i] = opaque((ax0 ^ (i << 5)) + 65536); }
    aw[0][0] = opaque(aw0); aw[0][1] = opaque((aw0 ^ 16) + 1024); aw[1][0] = opaque(aw0 + 65536); aw[1][1] = opaque((aw0 ^ 16) + 1024 + 65536);
    const u32 selv = (((l15 >> 2) ^ g4) & 1) ? 0u : 0x3f803f80u;
    u32x4 sel = {selv, selv, selv, selv};
    asm volatile("" : "+v"(sel));
    f32x16 acc0 = {}, acc1 = {};
    f32x4 cs4 = {};
    u32x4 fx0[2][4], fx1[2][4], fw[2][4];
#pragma unroll
    for (int it = 0; it <= 10; ++it) {
      const int cu = it & 1, pv = cu ^ 1, hi = (it >> 1) & 1;
      const u32 bo = cu * 32768;
      if (it < 10) {
        if (it == 0) { if (relay) VMCNT(14); else VMCNT(10); }
        else if (it < 4) { if (relay) VMCNT(13); else VMCNT(9); }
        else if (it < 8) VMCNT(8);
        else if (it == 8) VMCNT(4);
        else VMCNT(0);
        if (it < 7) next();
        BAR();
        SB();
      }
#pragma unroll
      for (int i = 0; i < 4; ++i) {
        if (it > 0) acc0 = mfma32(fw[pv][i], fx0[pv][i], acc0);
        SB();
        if (it < 10) fw[cu][i] = ldstr(aw[hi][0] + bo + i * 4096, aw[hi][1] + bo + i * 4096);
        SB();
        if (it > 0) acc1 = mfma32(fw[pv][i], fx1[pv][i], acc1);
        SB();
        if (it < 10) { fx0[cu][i] = lds128(ax[hi][i] + bo); fx1[cu][i] = lds128(ax[hi][i] + bo + 4096); }
        SB();
        if (it > 0) cs4 = mfma16(sel, fw[pv][i], cs4);
        SB();
        if (it < 7) piece((it + 3) & 3, i);
        SB();
      }
      if (it == 0) {
        VMCNT(12);
        row_stats();
        if (relay) {
          const u32 ta = opaque(L_RELAY + w * 4096 + (8 * g4 + q4) * 128 + 8 * p4);
          u32x4 v[4];
#pragma unroll
          for (int f = 0; f < 4; ++f) v[f] = ldstr(ta + 32 * f, ta + 32 * f + 512);
#pragma unroll
          for (int f = 0; f < 4; ++f) {
            const u32 ctile = 4 * rnb + f, cbk = (ctile * 205) >> 10, ct = ctile - cbk * 5;
            st_dev(v[f], rws, v16, (isw2 ? OFF_W2 : OFF_WO) + ((cbk * (isw2 ? 48 : 20) + rkb) * 5 + ct) * 1024);
          }
        }
        SB();
      }
      if (it == 4 && w == 3) set_flag(rws, OFF_F0C + f1idx * 4, tag);
      if (it == 7 && w < 3) glds_dev(rws, v16, w < 2 ? OFF_XS + rb * 2048 + w * 1024 : OFF_F0C, L_XS + w * 1024);
    }
    STAMP(1);
    u32x4 xs[2];
    {
      const u32 a = opaque(L_XS + (64 * wm + l31) * 16);
      xs[0] = lds128(a); xs[1] = lds128(a + 512);
#pragma unroll 1
      for (u32 it = 0; __builtin_expect(!__all(xs[0].y == tag && xs[0].w == tag && xs[1].y == tag && xs[1].w == tag), 0); ++it) {
        if (it > (1u << 16)) { __builtin_amdgcn_raw_buffer_store_b32(0xdeadu, rws, OFF_ERR, 0, 16); break; }
        xs[0] = ld_dev(rws, l31 * 16, OFF_XS + (128 * rb + 64 * wm) * 16);
        xs[1] = ld_dev(rws, l31 * 16, OFF_XS + (128 * rb + 64 * wm + 32) * 16);
        asm volatile("" ::: "memory");
      }
    }
    {
      const u32 c11 = pk(cs4[0], cs4[0]);
      const float c2 = cs4[0] - bflo(c11);
      const u32x4 ca = {hh ? 0u : c11, hh ? 0u : pk(c2, c2), 0u, 0u};
      acc0 = mfma32(ca, u32x4{xs[0].x, xs[0].x, 0u, 0u}, acc0);
      acc1 = mfma32(ca, u32x4{xs[1].x, xs[1].x, 0u, 0u}, acc1);
    }
    const float qs = cb < 5 ? 0.22360679774997896f * 1.4426950408889634f : 1.0f;
#pragma unroll
    for (int mt = 0; mt < 2; ++mt) {
      const float rs = __uint_as_float(xs[mt].z) * qs;
      u32 d[8];
#pragma unroll
      for (int u = 0; u < 8; ++u) d[u] = pk((mt ? acc1[2 * u] : acc0[2 * u]) * rs, (mt ? acc1[2 * u + 1] : acc0[2 * u + 1]) * rs);
      const u32 so = OFF_QKV + (16 * cb + 4 * wn) * PS + (128 * rb + 64 * wm + 32 * mt) * 16;
      st_dev(widen32(d[0], d[1], d[2], d[3]), rws, voffP, so);
      st_dev(widen32(d[4], d[5], d[6], d[7]), rws, voffP, so + 2 * PS);
    }
    STAMP(2);
    const u32x4 f = lds128(L_CHK + v16);
    wready = __all(f.x == tag && f.y == tag && f.z == tag && (f.w == tag || p4 == 3));
  } else {
    const u32 sidx = pr * 4 + (j - 28);
    f1idx = sidx * 16 + 15;
    const rsrc_t rw = mkrsrc(pw1, 640 * 3072);
    side_loads();
#pragma unroll 1
    for (u32 i = 0; i < 15; ++i) {
      const u32 c = 64 * i + lane, r = (c * 5462) >> 16;
      glds(rw, r * 3072 + (c - r * 12) * 16, 80 * w * 3072 + sidx * 192, w * 15360 + i * 1024);
    }
    if (tid < 32) *(LDS u32*)(size_t)(L_CTRL + tid * 4) = 0;
    VMCNT(15);
    row_stats();
    float cs[3] = {0.f, 0.f, 0.f};
    const u32 ta = w * 15360 + (8 * hh + q4) * 192 + 32 * blk + 8 * p4;
#pragma unroll 1
    for (u32 s = 0; s < 5; ++s) {
      VMCNT(13);
      u32x4 v[3];
#pragma unroll
      for (int ct = 0; ct < 3; ++ct) v[ct] = ldstr(ta + s * 3072 + 64 * ct, ta + s * 3072 + 64 * ct + 768);
#pragma unroll
      for (int ct = 0; ct < 3; ++ct) {
        st_dev(v[ct], rws, v16, OFF_W1 + ((sidx * 40 + 5 * w + s) * 3 + ct) * 1024);
        cs[ct] = sum8(v[ct], cs[ct]);
      }
    }
#pragma unroll
    for (int ct = 0; ct < 3; ++ct) {
      cs[ct] = halves_sum(cs[ct]);
      if (lane < 32) *(LDS float*)(size_t)(L_RELAY + w * 384 + (32 * ct + lane) * 4) = cs[ct];
    }
    BAR();
    if (tid < 24) {
      f32x4 t = {0.f, 0.f, 0.f, 0.f};
#pragma unroll
      for (int k = 0; k < 8; ++k) t += ldsf4(L_RELAY + k * 384 + tid * 16);
      st_dev(__builtin_bit_cast(u32x4, t), rws, tid * 16, OFF_CS1 + sidx * 384);
    }
  }
  DRAIN();
  STAMP(3);
  BAR();
  const u32 half = w >> 2, w4 = w & 3;
  const u32 cb3 = 4 * xo + (j & 3), r3 = 512 * pr + 256 * (j >> 4) + 32 * ((j >> 2) & 3);
  const u32 cb4 = 8 * xo + (j & 7);
  {
    const u32 head = 4 * xcd + (j & 3), c = j >> 2, odd = head & 1;
    const u32 pb = 5 * (head >> 1), plA = pb + 3 * odd, plC = pb + 2;
    if (w == 0) {
      set_flag(rws, OFF_F1 + f1idx * FS, tag);
      const u32 ty = lane >> 3;
      const int rbi = 2 * (int)c - 2 + (int)((lane >> 1) & 3);
      u32 fi = rbi * 16 + 5 * ty + ((pb + 2 * odd + 2 * (lane & 1)) >> 4);
      bool need = lane < 24 && rbi >= 0 && (ty > 0 || (lane & 4));
      if (lane == 24) { fi = cb4 * 16 + 15; need = true; }
      if (g4 == 2 && b == 0) { fi = l15 * 16 + 15; need = true; }
      poll1(rws, fi * FS, OFF_F1, tag, need);
    } else {
#pragma unroll 1
      for (u32 it = 0; __builtin_expect(!wready, 0); ++it) {
        const u32x4 f = ld_dev(rws, v16, OFF_F0C);
        asm volatile("" ::: "memory");
        wready = __all(f.x == tag && f.y == tag && f.z == tag && (f.w == tag || p4 == 3));
        if (it > (1u << 14)) { __builtin_amdgcn_raw_buffer_store_b32(0xdeadu, rws, OFF_ERR, 0, 16); break; }
        if (!wready) __builtin_amdgcn_s_sleep(16);
      }
#pragma unroll 1
      for (u32 i = w - 1; i < 100; i += 7) glds_dev(rws, v16, OFF_WO + (cb3 * 100 + i) * 1024, L_WO + i * 1024);
    }
    *(LDS u32x4*)(size_t)(6 * KVS + tid * 16) = u32x4{0x3f80u, 0u, (c == 0 && tid < 256) ? 0xf149u : 0u, 0u};
    BAR();
    STAMP(4);
    const u32 q0 = 256 * c + 32 * w;
    u32x4 q1 = ld_dev(rws, voffP, OFF_QKV + plA * PS + q0 * 16);
    u32x4 q2 = ld_dev(rws, l31 * 16, OFF_QKV + plC * PS + q0 * 16);
    {
      const u32 t0 = (c == 0 && w < 4) ? 64 * w : 256 * c - 256 + 64 * w;
      const u32 so = OFF_QKV + t0 * 16, l = w * 1024;
      glds_dev(rws, v16, so + (80 + plA) * PS, l);
      glds_dev(rws, v16, so + (81 + plA) * PS, l + KVS);
      glds_dev(rws, v16, so + (80 + plC) * PS, l + 2 * KVS);
      glds_dev(rws, v16, so + (160 + plA) * PS, l + 3 * KVS);
      glds_dev(rws, v16, so + (161 + plA) * PS, l + 4 * KVS);
      glds_dev(rws, v16, so + (160 + plC) * PS, l + 5 * KVS);
    }
    if (w == 7 && lane < 24) {
      const u32x4 cv = ld_dev(rws, v16, OFF_CS1 + cb4 * 384);
      const u32x2 bv = *(const u32x2*)((const char*)pb1 + cb4 * 192 + lane * 8);
      *(LDS u32x4*)(size_t)(L_TAB + v16) = cv;
      lds_st4(L_TAB + 384 + v16, f32x4{bflo(bv.x), bfhi(bv.x), bflo(bv.y), bfhi(bv.y)});
    }
    if (b == 0 && w == 0) set_flag(rws, OFF_EPOCH, tag);
    if (hh) q2 = u32x4{0u, 0u, 0x3f80u, 0u};
    else if (odd) { q2.x = 0u; q2.y = 0u; }
    else { q2.z = 0u; q2.w = 0u; }
    DRAIN();
    asm volatile("" : "+v"(q1), "+v"(q2));
    BAR();
    STAMP(5);
    if (!half) __builtin_amdgcn_s_setprio(1);
    touch(rws, lane < 30 ? lane * 128 : OOB, OFF_W1 + cb4 * 122880 + ((j >> 3) * 8 + w) * 3840);
    u32x4 w2f[6][5];
    const u32 w2o = OFF_W2 + (cb3 * 48 + 6 * w) * 5120;
    const u32 ka = hh * KVS + (32 * w + l31) * 16, kb = (hh ? 6 : 2) * KVS + (32 * w + l31) * 16;
    f32x16 s[9];
#pragma unroll
    for (int t = 0; t < 9; ++t) {
      const f32x16 z = {};
      s[t] = mfma32(lds128(ka + t * 512), q1, z);
      s[t] = mfma32(lds128(kb + t * 512), q2, s[t]);
    }
#pragma unroll
    for (int ct = 0; ct < 5; ++ct) w2f[0][ct] = ld_dev(rws, v16, w2o + ct * 1024);
    const int e = (int)l31 - 4 * (int)hh;
#pragma unroll
    for (int r = 0; r < 16; ++r) s[0][r] = (8 * (r >> 2) + (r & 3) > e) ? s[0][r] : s[8][r];
    float mt8[8];
#pragma unroll
    for (int t = 0; t < 8; ++t) {
      mt8[t] = fmaxf(s[t][0], s[t][1]);
#pragma unroll
      for (int r = 2; r < 16; r += 2) mt8[t] = fmaxf(fmaxf(mt8[t], s[t][r]), s[t][r + 1]);
    }
    float m = fmaxf(fmaxf(fmaxf(mt8[0], mt8[1]), fmaxf(mt8[2], mt8[3])), fmaxf(fmaxf(mt8[4], mt8[5]), fmaxf(mt8[6], mt8[7])));
    { auto r = __builtin_amdgcn_permlane32_swap(__float_as_uint(m), __float_as_uint(m), false, false); m = fmaxf(__uint_as_float(r[0]), __uint_as_float(r[1])); }
    const u32 va = (blk ? ((p4 & 1) ? 6 * KVS : 5 * KVS + 8 * odd) : (3 + (p4 >> 1)) * KVS + 8 * (p4 & 1)) + (32 * w + 4 * hh + q4) * 16;
    f32x16 o = {};
#pragma unroll
    for (int t = 0; t < 8; ++t) {
      SB();
      if (t < 5) { w2f[1 + (2 * t) / 5][(2 * t) % 5] = ld_dev(rws, v16, w2o + (5 + 2 * t) * 1024); w2f[1 + (2 * t + 1) / 5][(2 * t + 1) % 5] = ld_dev(rws, v16, w2o + (6 + 2 * t) * 1024); }
      float p[16];
#pragma unroll
      for (int r = 0; r < 16; ++r) p[r] = __builtin_amdgcn_exp2f(s[t][r] - m);
      if (t == 0) {
        float p8[16];
#pragma unroll
        for (int r = 0; r < 16; ++r) { const bool cnd = 8 * (r >> 2) + (r & 3) > e; p8[r] = cnd ? 0.f : p[r]; p[r] = cnd ? p[r] : 0.f; }
#pragma unroll
        for (int u = 0; u < 2; ++u)
          o = mfma32(ldstr(va + 4096 + u * 256, va + 4096 + u * 256 + 128),
                     u32x4{pk(p8[8 * u], p8[8 * u + 1]), pk(p8[8 * u + 2], p8[8 * u + 3]), pk(p8[8 * u + 4], p8[8 * u + 5]), pk(p8[8 * u + 6], p8[8 * u + 7])}, o);
      }
#pragma unroll
      for (int u = 0; u < 2; ++u)
        o = mfma32(ldstr(va + t * 512 + u * 256, va + t * 512 + u * 256 + 128),
                   u32x4{pk(p[8 * u], p[8 * u + 1]), pk(p[8 * u + 2], p[8 * u + 3]), pk(p[8 * u + 4], p[8 * u + 5]), pk(p[8 * u + 6], p[8 * u + 7])}, o);
    }
    float inv;
    { auto r = __builtin_amdgcn_permlane32_swap(__float_as_uint(o[8]), __float_as_uint(o[8]), false, false); inv = __builtin_amdgcn_rcpf(__uint_as_float(r[1])); }
    st_dev(widen32(pk(o[0] * inv, o[1] * inv), pk(o[2] * inv, o[3] * inv), pk(o[4] * inv, o[5] * inv), pk(o[6] * inv, o[7] * inv)), rws, voffP,
           OFF_O + plA * PS + q0 * 16);
    __builtin_amdgcn_raw_buffer_store_b64(u32x2{pk(o[8] * inv, o[9] * inv), pk(o[10] * inv, o[11] * inv)}, rws, hh ? OOB : v16, OFF_O + plC * PS + q0 * 16 + 8 * odd, 16);
    STAMP(6);
    u32x4 wf[5][5];
#pragma unroll
    for (int cc = 0; cc < 5; ++cc) {
      const u32 a = opaque(L_WO + w4 * 25600 + (cc < 4 ? ((w4 + cc) & 3) : 4) * 1024 + v16);
#pragma unroll
      for (int si = 0; si < 5; ++si) wf[si][cc] = lds128(a + si * 5120);
    }
    SB();
    lds_inc(C_FREE);
    DRAIN();
    set_flag(rws, OFF_F2 + ((q0 >> 5) * 32 + head) * FS, tag);
    STAMP(7);

    const u32 rr = r3 + 128 * half;
    f32x4 acc[2][5] = {};
    {
      u32x2 xr[2][2];
#pragma unroll
      for (int pp = 0; pp < 2; ++pp)
#pragma unroll
        for (int m2 = 0; m2 < 2; ++m2)
          xr[pp][m2] = __builtin_amdgcn_raw_buffer_load_b64(rx, (pp && w4) ? OOB : (16 * m2 + l15) * 1280 + g4 * 8, rr * 1280 + 160 * cb3 + 32 * (pp ? 4 : w4), 0);
      lds_wait(C_FREE, 8);
#pragma unroll
      for (int i = 0; i < 8; ++i) glds_pf(rws, v16, OFF_W1 + (cb4 * 120 + i * 8 + w) * 1024, (i * 8 + w) * 1024);
      poll1(rws, lane * FS, OFF_F2 + ((rr >> 5) * 32 + 8 * w4) * FS, tag, lane < 8);
      STAMP(8);
      u32x4 af[5][2];
#pragma unroll
      for (int si = 0; si < 5; ++si)
#pragma unroll
        for (int m2 = 0; m2 < 2; ++m2) af[si][m2] = ld_dev(rws, voffQ, OFF_O + (20 * w4 + 4 * si) * PS + (rr + 16 * m2) * 16);
      SB();
#pragma unroll
      for (int m2 = 0; m2 < 2; ++m2) {
        acc[m2][0] = f32x4{bflo(xr[0][m2].x), bfhi(xr[0][m2].x), bflo(xr[0][m2].y), bfhi(xr[0][m2].y)};
        acc[m2][4] = f32x4{bflo(xr[1][m2].x), bfhi(xr[1][m2].x), bflo(xr[1][m2].y), bfhi(xr[1][m2].y)};
      }
#pragma unroll
      for (int si = 0; si < 5; ++si)
#pragma unroll
        for (int m2 = 0; m2 < 2; ++m2)
#pragma unroll
          for (int cc = 0; cc < 5; ++cc) acc[m2][cc] = mfma16(wf[si][cc], af[si][m2], acc[m2][cc]);
    }
    STAMP(9);
    lds_wait(C_SCR, 4 * half);
    const u32 s0l = opaque(L_S0 + v16);
#pragma unroll
    for (int cc = 1; cc < 5; ++cc) {
      const u32 a = s0l + (cc < 4 ? (((w4 + cc) & 3) * 3 + cc - 1) * 2048 : 24576 + w4 * 2048);
      lds_st4(a, acc[0][cc]);
      lds_st4(a + 1024, acc[1][cc]);
    }
    lds_inc(C_X3 + half * 4);
    lds_wait(C_X3 + half * 4, 4);
    u32 hp[2][2][2];
#pragma unroll
    for (int pp = 0; pp < 2; ++pp) {
      if (pp == 0 || w4 == 0) {
        const u32 a = pp ? s0l + 24576 + 2048 : s0l + w4 * 6144;
#pragma unroll
        for (int m2 = 0; m2 < 2; ++m2) {
          const f32x4 v = (acc[m2][pp ? 4 : 0] + ldsf4(a + m2 * 1024)) + (ldsf4(a + 2048 + m2 * 1024) + ldsf4(a + 4096 + m2 * 1024));
          hp[pp][m2][0] = pk(v.x, v.y);
          hp[pp][m2][1] = pk(v.z, v.w);
        }
        st_dev(widen16(hp[pp][0][0], hp[pp][0][1], hp[pp][1][0], hp[pp][1][1]), rws, voffP, OFF_H + (10 * cb3 + 2 * (pp ? 4 : w4)) * PS + rr * 16);
      } else {
        hp[pp][0][0] = hp[pp][0][1] = hp[pp][1][0] = hp[pp][1][1] = 0u;
      }
    }
    lds_inc(C_SCR);
    STAMP(10);
    {
      float sv[2], qv[2];
#pragma unroll
      for (int m2 = 0; m2 < 2; ++m2) {
        sv[m2] = 0.f; qv[m2] = 0.f;
#pragma unroll
        for (int pp = 0; pp < 2; ++pp)
#pragma unroll
          for (int k = 0; k < 2; ++k) {
            const float a = bflo(hp[pp][m2][k]), c2 = bfhi(hp[pp][m2][k]);
            sv[m2] += a + c2; qv[m2] += a * a + c2 * c2;
          }
      }
      auto r1 = __builtin_amdgcn_permlane16_swap(__float_as_uint(sv[0]), __float_as_uint(sv[1]), false, false);
      auto r2 = __builtin_amdgcn_permlane16_swap(__float_as_uint(qv[0]), __float_as_uint(qv[1]), false, false);
      auto r3_ = __builtin_amdgcn_permlane32_swap(__float_as_uint(__uint_as_float(r1[0]) + __uint_as_float(r1[1])),
                                                  __float_as_uint(__uint_as_float(r2[0]) + __uint_as_float(r2[1])), false, false);
      *(LDS float*)(size_t)(L_HS + w * 256 + lane * 4) = __uint_as_float(r3_[0]) + __uint_as_float(r3_[1]);
    }
    if (lds_ticket(C_HS + half * 4) == 3) {
      const u32 a = opaque(L_HS + half * 1024 + lane * 4);
      const float t = (*(LDS float*)(size_t)a + *(LDS float*)(size_t)(a + 256)) + (*(LDS float*)(size_t)(a + 512) + *(LDS float*)(size_t)(a + 768));
      VMCNT(0);
      set_flag(rws, OFF_F3 + ((rr >> 5) * 32 + cb3 * 4 + w4) * FS, tag);
      __builtin_amdgcn_raw_buffer_store_b64(u32x2{__float_as_uint(t), tag}, rws, lane * 8, OFF_HS + ((rr >> 5) * 8 + cb3) * 512, 16);
    } else {
      VMCNT(0);
      set_flag(rws, OFF_F3 + ((rr >> 5) * 32 + cb3 * 4 + w4) * FS, tag);
    }
    STAMP(11);
#pragma unroll
    for (int i = 8; i < 15; ++i) glds_pf(rws, v16, OFF_W1 + (cb4 * 120 + i * 8 + w) * 1024, (i * 8 + w) * 1024);

    {
      const u32 kh = w & 1, r4 = 512 * pr + 256 * (j >> 4) + 128 * half + 64 * ((j >> 3) & 1) + 32 * ((w >> 1) & 1);
      poll1(rws, lane * FS, OFF_F3 + ((r4 >> 5) * 32 + 16 * kh) * FS, tag, lane < 16);
      STAMP(12);
      u32x4 hf[20];
#pragma unroll
      for (int i = 0; i < 20; ++i) hf[i] = ld_dev(rws, voffP, OFF_H + (40 * kh + 2 * i) * PS + r4 * 16);
      u32x2 hs[4];
#pragma unroll
      for (int k = 0; k < 4; ++k) hs[k] = __builtin_amdgcn_raw_buffer_load_b64(rws, lane * 8, OFF_HS + ((r4 >> 5) * 8 + 4 * kh + k) * 512, 16);
      VMCNT(24);
      lds_inc(C_W1);
      lds_wait(C_W1, 8);
      f32x16 a4[3] = {};
      const u32 wa = kh * 61440 + (lane ^ (kh << 4)) * 16;
      u32x4 ring[6];
#pragma unroll
      for (int n = 0; n < 6; ++n) ring[n] = lds128(wa + n * 1024);
#pragma unroll
      for (int n = 0; n < 60; ++n) {
        SB();
        a4[n % 3] = mfma32(ring[n % 6], hf[n / 3], a4[n % 3]);
        if (n + 6 < 60) ring[n % 6] = lds128(wa + (n + 6) * 1024);
      }
      SB();
      STAMP(13);
      lds_inc(C_FREE2);
#pragma unroll 1
      for (u32 it = 0; __builtin_expect(!__all(hs[0].y == tag && hs[1].y == tag && hs[2].y == tag && hs[3].y == tag), 0); ++it) {
        if (it > (1u << 16)) { __builtin_amdgcn_raw_buffer_store_b32(0xdeadu, rws, OFF_ERR, 0, 16); break; }
#pragma unroll
        for (int k = 0; k < 4; ++k) hs[k] = __builtin_amdgcn_raw_buffer_load_b64(rws, lane * 8, OFF_HS + ((r4 >> 5) * 8 + 4 * kh + k) * 512, 16);
        asm volatile("" ::: "memory");
      }
      const float myst = (__uint_as_float(hs[0].x) + __uint_as_float(hs[1].x)) + (__uint_as_float(hs[2].x) + __uint_as_float(hs[3].x));
#pragma unroll
      for (int n = 15; n < 23; ++n) w2f[n / 5][n % 5] = ld_dev(rws, v16, w2o + n * 1024);
      lds_wait(C_SCR, 8 + 4 * half);
      {
        const u32 a = s0l + w4 * 6144;
#pragma unroll
        for (int ct = 0; ct < 3; ++ct) {
          lds_st4(a + ct * 2048, f32x4{a4[ct][8], a4[ct][9], a4[ct][10], a4[ct][11]});
          lds_st4(a + ct * 2048 + 1024, f32x4{a4[ct][12], a4[ct][13], a4[ct][14], a4[ct][15]});
        }
      }
      *(LDS float*)(size_t)(L_ST4 + w * 256 + lane * 4) = myst;
      lds_inc(C_X4 + (w >> 1) * 4);
      lds_wait(C_X4 + (w >> 1) * 4, 2);
      const float tot = myst + *(LDS float*)(size_t)(L_ST4 + (w ^ 1) * 256 + lane * 4);
      const auto rt = __builtin_amdgcn_permlane32_swap(__float_as_uint(tot), __float_as_uint(tot), false, false);
      const float sm = __uint_as_float(rt[0]), sq = __uint_as_float(rt[1]);
      const float mu = sm * (1.0f / 640.0f);
      const float var = fmaxf(sq * (1.0f / 640.0f) - mu * mu, 0.f);
      const float r1 = __builtin_amdgcn_rsqf(var + 1e-5f);
      const float rs = r1 * __builtin_amdgcn_rsqf(var * r1 * r1 + 1e-5f);
      const float nmr = -mu * rs;
      const u32 oa = s0l + (w4 ^ 1) * 6144, ta = opaque(L_TAB + (16 * kh + 4 * hh) * 4);
#pragma unroll
      for (int ct = 0; ct < 3; ++ct) {
        u32 d[4];
#pragma unroll
        for (int g = 0; g < 2; ++g) {
          const f32x4 ot = ldsf4(oa + ct * 2048 + g * 1024);
          const f32x4 c4 = ldsf4(ta + ct * 128 + g * 32), bb = ldsf4(ta + 384 + ct * 128 + g * 32);
          float y[4];
#pragma unroll
          for (int r = 0; r < 4; ++r) {
            const float u = (a4[ct][4 * g + r] + ot[r]) * rs + (nmr * c4[r] + bb[r]);
            const float ex = __builtin_amdgcn_exp2f(u * (-2.302208198f - 0.1029432397f * u * u));
            y[r] = u * __builtin_amdgcn_rcpf(1.0f + ex);
          }
          d[2 * g] = pk(y[0], y[1]);
          d[2 * g + 1] = pk(y[2], y[3]);
        }
        st_dev(widen32(d[0], d[1], d[2], d[3]), rws, voffP, OFF_H1 + (12 * cb4 + 4 * ct + 2 * kh) * PS + r4 * 16);
      }
      lds_inc(C_SCR);
      STAMP(14);
      DRAIN();
      set_flag(rws, OFF_F4 + ((r4 >> 5) * 32 + cb4 * 2 + kh) * FS, tag);
      STAMP(15);
    }
#pragma unroll
    for (int n = 23; n < 30; ++n) w2f[n / 5][n % 5] = ld_dev(rws, v16, w2o + n * 1024);
    u32x2 b2r[2];
#pragma unroll
    for (int pp = 0; pp < 2; ++pp) b2r[pp] = *(const u32x2*)((const char*)pb2 + 160 * cb3 + 32 * (pp ? 4 : w4) + 8 * g4);
    __builtin_amdgcn_s_setprio(0);

    const rsrc_t ro = mkrsrc(pout, 2048 * 1280);
    u32x4 af[6][2];
    poll1(rws, lane * FS, OFF_F4 + ((r3 >> 5) * 32 + 4 * w) * FS, tag, lane < 4);
    STAMP(16);
#pragma unroll
    for (int si = 0; si < 6; ++si)
#pragma unroll
      for (int m2 = 0; m2 < 2; ++m2) af[si][m2] = ld_dev(rws, voffQ, OFF_H1 + (24 * w + 4 * si) * PS + (r3 + 16 * m2) * 16);
    SB();
#pragma unroll
    for (int X = 0; X < 2; ++X) {
      f32x4 a5[2][5] = {};
#pragma unroll
      for (int si = 0; si < 6; ++si)
#pragma unroll
        for (int m2 = 0; m2 < 2; ++m2)
#pragma unroll
          for (int ct = 0; ct < 5; ++ct) a5[m2][ct] = mfma16(w2f[si][ct], af[si][m2], a5[m2][ct]);
      if (X == 0) {
        STAMP(17);
        poll1(rws, lane * FS, OFF_F4 + (((r3 + 128) >> 5) * 32 + 4 * w) * FS, tag, lane < 4);
        STAMP(18);
#pragma unroll
        for (int si = 0; si < 6; ++si)
#pragma unroll
          for (int m2 = 0; m2 < 2; ++m2) af[si][m2] = ld_dev(rws, voffQ, OFF_H1 + (24 * w + 4 * si) * PS + (r3 + 128 + 16 * m2) * 16);
        lds_wait(C_FREE2, 8);
      } else {
        STAMP(20);
      }
      {
        const u32 a0 = opaque(X * 81920 + w * 1024 + v16), a1 = opaque(X * 81920 + 49152 + w * 1024 + v16);
#pragma unroll
        for (int ct = 0; ct < 5; ++ct)
#pragma unroll
          for (int m2 = 0; m2 < 2; ++m2) lds_st4(ct < 3 ? a0 + (ct * 2 + m2) * 8192 : a1 + (ct * 2 + m2 - 6) * 8192, a5[m2][ct]);
      }
      BAR();
      if (X == 0) STAMP(19); else STAMP(21);
      if (half == X) {
#pragma unroll
        for (int pp = 0; pp < 2; ++pp) {
          if (pp == 0 || w4 == 0) {
            const u32 ct = pp ? 4 : w4;
            const u32 a = opaque(X * 81920 + ct * 16384 + v16);
            u32 d[2][2];
#pragma unroll
            for (int m2 = 0; m2 < 2; ++m2) {
              f32x4 t[8];
#pragma unroll
              for (int k = 0; k < 8; ++k) t[k] = ldsf4(a + m2 * 8192 + k * 1024);
              f32x4 v = {bflo(hp[pp][m2][0]) + bflo(b2r[pp].x), bfhi(hp[pp][m2][0]) + bfhi(b2r[pp].x), bflo(hp[pp][m2][1]) + bflo(b2r[pp].y),
                         bfhi(hp[pp][m2][1]) + bfhi(b2r[pp].y)};
              v += ((t[0] + t[1]) + (t[2] + t[3])) + ((t[4] + t[5]) + (t[6] + t[7]));
              d[m2][0] = pk(v.x, v.y);
              d[m2][1] = pk(v.z, v.w);
            }
            st_plain(widen16(d[0][0], d[0][1], d[1][0], d[1][1]), ro, l31 * 1280 + hh * 16, rr * 1280 + (80 * cb3 + 16 * ct) * 2);
          }
        }
      }
    }
  }
}

extern "C" void kernel_launch(void* const* d_in, const int* in_sizes, int n_in, void* d_out, int out_size, void* d_ws, size_t ws_size,
                              hipStream_t stream) {
  fused_block<<<256, 512, 0, stream>>>(d_in[0], d_in[1], d_in[2], d_in[4], d_in[5], d_in[6], d_in[7], d_out, d_ws);
}
```

```cpp
#include <hip/hip_runtime.h>

typedef unsigned int u32;
typedef unsigned long long u64;
typedef __bf16 bf16x2 __attribute__((ext_vector_type(2)));
typedef __bf16 bf16x8 __attribute__((ext_vector_type(8)));
typedef short s16x4 __attribute__((ext_vector_type(4)));
typedef float f32x2 __attribute__((ext_vector_type(2)));
typedef float f32x4 __attribute__((ext_vector_type(4)));
typedef float f32x16 __attribute__((ext_vector_type(16)));
typedef u32 u32x2 __attribute__((ext_vector_type(2)));
typedef u32 u32x4 __attribute__((ext_vector_type(4)));
typedef __amdgpu_buffer_rsrc_t rsrc_t;
#define LDS __attribute__((address_space(3)))
#define DI __device__ __forceinline__

constexpr u32 GS_QKV = 240 * 512, GS_O = 80 * 512, GS_H1 = 192 * 512;
constexpr u32 OFF_EPOCH = 0, OFF_ERR = 128, OFF_SEEN = 1024, OFF_XS = 16384, OFF_CSQ = 65536;
constexpr u32 OFF_MB1 = OFF_CSQ + 160 * 1024, OFF_MB2 = OFF_MB1 + 256 * 128, OFF_MB3 = OFF_MB2 + 2048 * 128, OFF_MB4 = OFF_MB3 + 2048 * 128;
constexpr u32 OFF_MB0 = OFF_MB4 + 4096 * 128, OFF_MBW = OFF_MB0 + 256 * 128, OFF_HS = OFF_MBW + 272 * 128;
constexpr u32 OFF_QKV = OFF_HS + 512 * 512, OFF_O = OFF_QKV + 64 * GS_QKV, OFF_H = OFF_O + 64 * GS_O, OFF_H1 = OFF_H + 64 * GS_O;
constexpr u32 OFF_WO = OFF_H1 + 64 * GS_H1, OFF_W1 = OFF_WO + 800 * 1024, OFF_W2 = OFF_W1 + 1920 * 1024;
constexpr u32 OFF_CSP = OFF_W2 + 1920 * 1024;
constexpr u32 WS_END = OFF_CSP + 20 * 6144;
#define OFF_ERR_DEF OFF_ERR
#define HAVE_PLANES 2
#define STAMP(i)

constexpr u32 L_XROW = 147456, L_JUNK = 162816;
constexpr u32 KVS = 8256, L_WO = 57856, L_RELAY = L_WO + 84 * 1024;
constexpr u32 L_S0 = 122880, L_HS = 155648, L_AUG = 160256;
constexpr u32 L_CTRL = 163584;
constexpr u32 C_FREE = L_CTRL, C_X3 = L_CTRL + 4, C_SCR = L_CTRL + 12, C_W1 = L_CTRL + 16, C_X4 = L_CTRL + 20, C_FREE2 = L_CTRL + 36, C_HS = L_CTRL + 40, C_WO = L_CTRL + 48;
#define DELAY4 4
#define SPARE_SLEEP 40
#define PSLEEP 6
#define RSLEEP 4
constexpr u32 CODE_BYTES = 32768;

template <int N> struct IC { static constexpr int v = N; };
DI rsrc_t mkrsrc(const void* p, u32 bytes) { return __builtin_amdgcn_make_buffer_rsrc((void*)p, (short)0, (int)bytes, 0x00020000); }
DI void glds(rsrc_t rs, u32 voff, u32 soff, u32 l) {
  asm volatile("s_mov_b32 m0, %0\n\ts_nop 0\n\tbuffer_load_dwordx4 %1, %2, %3 offen lds" ::"s"(l), "v"(voff), "s"(rs), "s"(soff) : "memory");
}
DI void glds_nt(rsrc_t rs, u32 voff, u32 soff, u32 l) {
  asm volatile("s_mov_b32 m0, %0\n\ts_nop 0\n\tbuffer_load_dwordx4 %1, %2, %3 offen nt lds" ::"s"(l), "v"(voff), "s"(rs), "s"(soff) : "memory");
}
DI void glds_dev(rsrc_t rs, u32 voff, u32 soff, u32 l) {
  asm volatile("s_mov_b32 m0, %0\n\ts_nop 0\n\tbuffer_load_dwordx4 %1, %2, %3 offen sc1 lds" ::"s"(l), "v"(voff), "s"(rs), "s"(soff) : "memory");
}
DI void glds4(rsrc_t rs, u32 voff, u32 soff, u32 l) {
  asm volatile("s_mov_b32 m0, %0\n\ts_nop 0\n\tbuffer_load_dwordx4 %1, %2, %3 offen sc1 lds\n\tbuffer_load_dwordx4 %1, %2, %3 offen offset:1024 sc1 lds\n\t"
               "buffer_load_dwordx4 %1, %2, %3 offen offset:2048 sc1 lds\n\tbuffer_load_dwordx4 %1, %2, %3 offen offset:3072 sc1 lds" ::"s"(l), "v"(voff), "s"(rs), "s"(soff));
}
DI void glds3(rsrc_t rs, u32 voff, u32 soff, u32 l) {
  asm volatile("s_mov_b32 m0, %0\n\ts_nop 0\n\tbuffer_load_dwordx4 %1, %2, %3 offen sc1 lds\n\tbuffer_load_dwordx4 %1, %2, %3 offen offset:1024 sc1 lds\n\t"
               "buffer_load_dwordx4 %1, %2, %3 offen offset:2048 sc1 lds" ::"s"(l), "v"(voff), "s"(rs), "s"(soff));
}
DI void touch(rsrc_t rs, u32 voff, u32 soff) {
  asm volatile("s_mov_b32 m0, %0\n\ts_nop 0\n\tbuffer_load_dword %1, %2, %3 offen sc1 lds" ::"s"(L_JUNK), "v"(voff), "s"(rs), "s"(soff));
}
DI u32x4 ld_dev(rsrc_t rs, u32 voff, u32 soff) { return __builtin_amdgcn_raw_buffer_load_b128(rs, voff, soff, 16); }
DI u32x4 ld_at(rsrc_t rs, u32 voff, u32 soff, u32 c) { return ld_dev(rs, voff + (c & 4095), soff + (c & ~4095u)); }
DI u32x4 ld_raw(rsrc_t rs, u32 voff, u32 soff) {
  u32x4 r;
  asm volatile("buffer_load_dwordx4 %0, %1, %2, %3 offen sc1" : "=&v"(r) : "v"(voff), "s"(rs), "s"(soff) : "memory");
  return r;
}
DI void st_dev(u32x4 v, rsrc_t rs, u32 voff, u32 soff) {
  asm volatile("buffer_store_dwordx4 %0, %1, %2, %3 offen sc1\n\ts_nop 1" ::"v"(v), "v"(voff), "s"(rs), "s"(soff) : "memory");
}
DI void st_plain(u32x4 v, rsrc_t rs, u32 voff, u32 soff) {
  asm volatile("buffer_store_dwordx4 %0, %1, %2, %3 offen\n\ts_nop 1" ::"v"(v), "v"(voff), "s"(rs), "s"(soff) : "memory");
}
DI u32x4 lds128(u32 a) { return *(LDS u32x4*)(size_t)a; }
DI f32x4 ldsf4(u32 a) { return *(LDS f32x4*)(size_t)a; }
DI void lds_st4(u32 a, f32x4 v) { *(LDS f32x4*)(size_t)a = v; }
DI u32x4 ldstr(u32 a0, u32 a1) {
  u32x2 lo = __builtin_bit_cast(u32x2, __builtin_amdgcn_ds_read_tr16_b64_v4i16((LDS s16x4*)(size_t)a0));
  u32x2 hi = __builtin_bit_cast(u32x2, __builtin_amdgcn_ds_read_tr16_b64_v4i16((LDS s16x4*)(size_t)a1));
  return u32x4{lo.x, lo.y, hi.x, hi.y};
}
DI f32x16 mfma32(u32x4 a, u32x4 b, f32x16 c) { return __builtin_amdgcn_mfma_f32_32x32x16_bf16(__builtin_bit_cast(bf16x8, a), __builtin_bit_cast(bf16x8, b), c, 0, 0, 0); }
DI f32x4 mfma16(u32x4 a, u32x4 b, f32x4 c) { return __builtin_amdgcn_mfma_f32_16x16x32_bf16(__builtin_bit_cast(bf16x8, a), __builtin_bit_cast(bf16x8, b), c, 0, 0, 0); }
DI float dot2(u32 a, u32 b, float c) { return __builtin_amdgcn_fdot2_f32_bf16(__builtin_bit_cast(bf16x2, a), __builtin_bit_cast(bf16x2, b), c, false); }
DI float sum8(u32x4 v, float c) { c = dot2(v.x, 0x3f803f80u, c); c = dot2(v.y, 0x3f803f80u, c); c = dot2(v.z, 0x3f803f80u, c); return dot2(v.w, 0x3f803f80u, c); }
DI u32 pk(float a, float b) { f32x2 v = {a, b}; return __builtin_bit_cast(u32, __builtin_convertvector(v, bf16x2)); }
DI float bflo(u32 v) { return __uint_as_float(v << 16); }
DI float bfhi(u32 v) { return __uint_as_float(v & 0xffff0000u); }
DI float halves_sum(float x) { auto r = __builtin_amdgcn_permlane32_swap(__float_as_uint(x), __float_as_uint(x), false, false); return __uint_as_float(r[0]) + __uint_as_float(r[1]); }
DI u32x4 widen32(u32 a0, u32 a1, u32 b0, u32 b1) {
  auto r0 = __builtin_amdgcn_permlane32_swap(a0, b0, false, false);
  auto r1 = __builtin_amdgcn_permlane32_swap(a1, b1, false, false);
  return u32x4{r0[0], r1[0], r0[1], r1[1]};
}
DI u32x4 widen16(u32 a0, u32 a1, u32 b0, u32 b1) {
  auto r0 = __builtin_amdgcn_permlane16_swap(a0, b0, false, false);
  auto r1 = __builtin_amdgcn_permlane16_swap(a1, b1, false, false);
  return u32x4{r0[0], r1[0], r0[1], r1[1]};
}
#define VMCNT(n) asm volatile("s_waitcnt vmcnt(" #n ")" ::: "memory")
#define SB() __builtin_amdgcn_sched_barrier(0)
#define OOB 0x7ffffff0u
#define BAR() do { asm volatile("s_waitcnt lgkmcnt(0)" ::: "memory"); __builtin_amdgcn_s_barrier(); asm volatile("" ::: "memory"); } while (0)
#define DRAIN() do { __builtin_amdgcn_s_waitcnt(0x0F70); asm volatile("s_waitcnt vmcnt(0)" ::: "memory"); } while (0)
DI u32 opaque(u32 v) { asm volatile("" : "+v"(v)); return v; }
DI u32 sopaque(u32 v) { asm volatile("" : "+s"(v)); return v; }
DI void lds_inc(u32 a) { asm volatile("s_mov_b64 exec, 1\n\tds_add_u32 %0, %1\n\ts_mov_b64 exec, -1" ::"v"(a), "v"(1u) : "memory"); }
DI u32 lds_ticket(u32 a) {
  u32 r;
  asm volatile("s_mov_b64 exec, 1\n\tds_add_rtn_u32 %0, %1, %2\n\ts_mov_b64 exec, -1\n\ts_waitcnt lgkmcnt(0)" : "=&v"(r) : "v"(a), "v"(1u) : "memory");
  return __builtin_amdgcn_readfirstlane(r);
}
DI void set_flag(rsrc_t rws, u32 off, u32 tag) {
  asm volatile("s_mov_b64 exec, 1\n\tbuffer_store_dword %0, off, %1, %2 sc1\n\ts_mov_b64 exec, -1" ::"v"(tag), "s"(rws), "s"(off) : "memory");
}
DI void lds_wait(u32 a, u32 target) {
  u32 t, n;
  asm volatile("s_mov_b32 %1, 0x100000\n"
               "1:\n\tds_read_b32 %0, %2\n\ts_waitcnt lgkmcnt(0)\n\tv_cmp_le_u32 vcc, %3, %0\n\ts_cbranch_vccnz 2f\n\t"
               "s_sub_u32 %1, %1, 1\n\ts_cbranch_scc0 1b\n"
               "2:" : "=&v"(t), "=&s"(n) : "v"(a), "s"(target) : "vcc", "scc", "memory");
}
DI void give_up(rsrc_t rws) { __builtin_amdgcn_raw_buffer_store_b32(0xdeadu, rws, OFF_ERR, 0, 16); }
DI void post(rsrc_t rws, u32 voff, u32 soff, u32 tag) {
  asm volatile("buffer_store_dword %0, %1, %2, %3 offen sc1" ::"v"(tag), "v"(voff), "s"(rws), "s"(soff) : "memory");
}
DI void await(rsrc_t rws, u32 v4, u32 soff, u32 tag, u64 mask) {
  u32 f, n;
  asm volatile("s_mov_b32 %1, 0x10000\n\ts_mov_b64 exec, %6\n"
               "1:\n\tv_mov_b32 %0, 0\n\tbuffer_atomic_or %0, %2, %3, %4 offen sc0 sc1\n\ts_waitcnt vmcnt(0)\n\tv_cmp_ne_u32 vcc, %5, %0\n\ts_cbranch_vccz 2f\n\t"
               "s_sub_u32 %1, %1, 1\n\ts_cbranch_scc0 1b\n"
               "2:\n\ts_mov_b64 exec, -1" : "=&v"(f), "=&s"(n) : "v"(v4), "s"(rws), "s"(soff), "s"(tag), "s"(mask) : "vcc", "scc", "memory");
  __builtin_amdgcn_s_waitcnt(0x0F70);
}
template <int CTRL> DI float dpp(float x) { return __builtin_bit_cast(float, __builtin_amdgcn_mov_dpp(__builtin_bit_cast(int, x), CTRL, 0xf, 0xf, true)); }
DI float wave_sum(float x) {
  x += dpp<0xB1>(x); x += dpp<0x4E>(x); x += dpp<0x141>(x); x += dpp<0x128>(x);
  auto r = __builtin_amdgcn_permlane16_swap(__float_as_uint(x), __float_as_uint(x), false, false);
  return halves_sum(__uint_as_float(r[0]) + __uint_as_float(r[1]));
}

__global__ __launch_bounds__(512, 2) void fused_block(const void* __restrict__ px, const void* __restrict__ pwqkv, const void* __restrict__ pwo,
                                                      const void* __restrict__ pw1, const void* __restrict__ pb1, const void* __restrict__ pw2,
                                                      const void* __restrict__ pb2, void* __restrict__ pout, void* pws) {
  __shared__ __attribute__((aligned(16))) char smem[163840];
  STAMP(0);
  asm volatile("" : "+s"(px), "+s"(pwqkv), "+s"(pwo), "+s"(pw1), "+s"(pb1), "+s"(pw2), "+s"(pb2), "+s"(pout), "+s"(pws));
  u32 tag, epoch;
  asm volatile("s_load_dword %0, %1, 0x0" : "=s"(epoch) : "s"(pws));
  const u32 tid = threadIdx.x, lane = tid & 63;
  const u32 w = __builtin_amdgcn_readfirstlane(tid >> 6);
  const u32 b = blockIdx.x, xcd = b & 7, j = b >> 3, pr = xcd >> 1, xo = xcd & 1;
  const u32 hh = lane >> 5, l31 = lane & 31, l15 = lane & 15, g4 = lane >> 4, q4 = (lane & 15) >> 2, p4 = lane & 3, blk = (lane >> 4) & 1;
  const rsrc_t rx = mkrsrc(px, 2048 * 1280);
  const rsrc_t rws = mkrsrc(pws, WS_END);
  asm volatile("" ::"s"((u32)(size_t)(LDS char*)smem));
  const u32 v16 = lane * 16, v4 = lane * 4;

  const bool spare = xo && j >= 28;
  const u32 cb3 = 4 * xo + (j & 3), g3 = 16 * pr + 8 * (j >> 4) + ((j >> 2) & 3);
  const u32 cb4 = 8 * xo + (j & 7);
  const u32 half = w >> 2, w4 = w & 3, gr = g3 + 4 * half;
  const u32 head = 4 * xcd + (j & 3), c = j >> 2, odd = head & 1;
  const u32 pb = 5 * (head >> 1), plA = pb + 3 * odd, plC = pb + 2;
  const u32 kh = w & 1, g4r = 16 * pr + 8 * (j >> 4) + 4 * half + 2 * ((j >> 3) & 1) + ((w >> 1) & 1);
  const u32 mb1 = OFF_MB1 + (c * 32 + head) * 128, mb2 = OFF_MB2 + ((gr * 8 + cb3) * 4 + w4) * 128, mb3 = OFF_MB3 + ((g4r * 16 + cb4) * 2 + kh) * 128,
            mb4 = OFF_MB4 + ((g3 * 8 + cb3) * 8 + w) * 128;
  const u32 selv = (((l15 >> 2) ^ g4) & 1) ? 0u : 0x3f803f80u;
  const u32x4 sel = {selv, selv, selv, selv};
  bool woready = false;
  u32 post1 = OOB;
  const u32 xrow = 512 * pr + (xo * 32 + j) * 8 + w;
  auto side_loads = [&]() {
    if (w == 7) {
      u64 pc;
      asm volatile("s_getpc_b64 %0" : "=s"(pc));
      const rsrc_t rc = mkrsrc((const void*)(pc & ~127ull), CODE_BYTES);
      asm volatile("s_mov_b32 m0, %0\n\ts_nop 0\n\tbuffer_load_dword %1, %2, 0 offen lds" ::"s"(L_JUNK), "v"(lane < 8 ? (j * 8 + lane) * 128 : OOB), "s"(rc) : "memory");
    }
    glds(rx, v16, xrow * 1280, L_XROW + w * 1280);
    asm volatile("s_mov_b32 m0, %0\n\ts_nop 0\n\tbuffer_load_dword %1, %2, %3 offen lds" ::"s"(L_XROW + w * 1280 + 1024), "v"(v4), "s"(rx), "s"(xrow * 1280 + 1024) : "memory");
  };
  auto row_stats = [&]() {
    asm volatile("s_waitcnt lgkmcnt(0)\n\ts_mul_i32 %0, %1, 0x9e3779b1\n\ts_add_u32 %0, %0, 0x7f4a7c15" : "=&s"(tag) : "s"(epoch) : "scc", "memory");
    float v[10], sum = 0.f;
    const u32 a = opaque(L_XROW + w * 1280 + v4);
#pragma unroll
    for (int k = 0; k < 5; ++k) {
      const u32 d = *(LDS u32*)(size_t)(a + k * 256);
      v[2 * k] = bflo(d); v[2 * k + 1] = bfhi(d);
      sum += v[2 * k] + v[2 * k + 1];
    }
    const float mu = wave_sum(sum) * (1.0f / 640.0f);
    float sq = 0.f;
#pragma unroll
    for (int k = 0; k < 10; ++k) sq += (v[k] - mu) * (v[k] - mu);
    const float rstd = __builtin_amdgcn_rsqf(wave_sum(sq) * (1.0f / 640.0f) + 1e-5f);
    const float m1 = bflo(pk(-mu, 0.f));
    st_dev(u32x4{pk(m1, -mu - m1), tag, __float_as_uint(rstd), tag}, rws, lane == 0 ? 0u : OOB, OFF_XS + xrow * 16);
    u32 wl = OFF_O + (8 * c + w) * GS_O + plC * 512 + p4 * 128;
    asm volatile("v_writelane_b32 %0, %1, 4\n\tv_writelane_b32 %0, %2, 5\n\tv_writelane_b32 %0, %3, 6\n\tv_writelane_b32 %0, %4, 7\n\tv_writelane_b32 %0, %5, 8"
                 : "+v"(wl) : "s"(mb2), "s"(mb3), "s"(mb4), "s"(mb4 + 32768), "s"(w ? OFF_MB0 + b * 128 : mb1));
    asm volatile("s_mov_b64 exec, %3\n\tbuffer_atomic_or %0, %1, %2, 0 offen sc1\n\ts_mov_b64 exec, -1" ::"v"(0u), "v"(wl), "s"(rws), "s"(w < 2 ? 0x1ffull : 0xffull) : "memory");
  };
  if (__builtin_expect(!spare, 1)) {
    const u32 rl = (j * (32 + 5 * xo)) >> 8, cl = j - rl * (8 - xo), cb = 8 * xo + cl, rb = 4 * pr + rl;
    const rsrc_t rw = mkrsrc(pwqkv, 640 * 3840);
    const u32 voffA = (8 * w + (lane >> 3)) * 1280 + (((lane & 7) ^ ((4 * w + g4) & 7)) << 4);
    const u32 voffB = (4 * w + g4) * 3840 + ((l15 ^ ((g4 << 2) | (w & 3))) << 4);
    u32 sk = ((cl * 5) >> 2) + ((rl * 5) >> 1) + pr;
    sk = sk >= 10 ? sk - 10 : sk;
    u32 sa = 0, sb = 0;
    auto next = [&]() { sa = rb * 163840 + sk * 128; sb = cb * 256 + sk * 245760; sk = sk == 9 ? 0 : sk + 1; };
    auto piece = [&](int buf, int i) {
      const u32 l = buf * 32768 + w * 1024;
      if (i == 0) glds(rx, voffA, sa, l);
      if (i == 1) glds(rw, voffB, sb, l + 16384);
      if (i == 2) glds(rx, voffA, sa + 81920, l + 8192);
      if (i == 3) glds(rw, voffB, sb + 122880, l + 24576);
    };
    auto stage = [&](int buf) { next(); piece(buf, 0); piece(buf, 1); piece(buf, 2); piece(buf, 3); };
    const u32 sk0 = sk;
    side_loads();
    stage(0);
    if (tid < 32) *(LDS u32*)(size_t)(L_CTRL + tid * 4) = 0;
    const u32 wm = w >> 2, wn = w & 3;
    const u32 ax0 = (64 * wm + l31) * 128 + ((hh ^ ((l31 >> 1) & 7)) << 4);
    const u32 aw0 = 16384 + (8 * hh + q4) * 256 + (((4 * wn + 2 * blk + (p4 >> 1)) ^ ((q4 << 2) | (2 * hh))) << 4) + 8 * (p4 & 1);
    u32 ax[2][4], aw[2][2];
#pragma unroll
    for (int i = 0; i < 4; ++i) { ax[0][i] = opaque(ax0 ^ (i << 5)); ax[1][i] = opaque((ax0 ^ (i << 5)) + 65536); }
    aw[0][0] = opaque(aw0); aw[0][1] = opaque((aw0 ^ 16) + 1024); aw[1][0] = opaque(aw0 + 65536); aw[1][1] = opaque((aw0 ^ 16) + 1024 + 65536);
    const u32 xsa = opaque(L_XROW + 10240 + (64 * wm + l31) * 16), csa = opaque(L_XROW + hh * 5120 + (32 * wn + l31) * 8), chka = opaque(L_XROW + 12288 + l15 * 4);
    const u32 so1 = sopaque(OFF_QKV + (4 * rb + 2 * wm) * GS_QKV + (16 * cb + 4 * wn) * 512);
    const u32 csv = opaque((wm || hh) ? OOB : lane * 8), cso = sopaque(OFF_CSQ + (cb * 10 + sk0) * 1024 + wn * 256);
    const u32 fso = sopaque(w < 5 ? OFF_CSQ + cb * 10240 + w * 2048 : w == 5 ? OFF_XS + rb * 2048 : OFF_MBW + b * 128);
    const u32 fv = opaque((w < 6 || lane < 8) ? v16 : OOB), fv2 = opaque(w < 6 ? v16 : OOB), fl = sopaque(L_XROW + (w < 7 ? w : 6) * 2048), wo1 = sopaque((8 + 8 * w) * 1024);
    {
      const u32 ty = (cb * 205) >> 10, cbl = cb - 5 * ty, hd = ((cbl * 32 * 205) >> 10) + (lane & 7), first = (5 * hd) >> 1, nx = (lane >> 3) & 1, cc = (rb >> 1) + nx;
      const bool val = lane < 16 && hd < 32 && first < 16 * cbl + 16 && cc < 8 && (ty > 0 || nx == 0);
      post1 = opaque(val ? (cc * 32 + hd) * 128 + (ty * 8 + ((rb & 1) + 2 - 2 * nx) * 2 + (cbl - (first >> 4))) * 4 : OOB);
    }
    VMCNT(4);
    row_stats();
    stage(1);
    f32x16 acc0 = {}, acc1 = {};
    f32x4 cs4 = {};
    float rs[2];
    u32x4 fx0[2][4], fx1[2][4], fw[2][4], xs[2], ca;
    u32x2 cg[5];
    u32 bad;
    float cs;
    auto prepA = [&]() {
      bad = (xs[0].y ^ tag) | (xs[0].w ^ tag) | (xs[1].y ^ tag) | (xs[1].w ^ tag);
#pragma unroll
      for (int k = 0; k < 5; ++k) bad |= cg[k].y ^ tag;
    };
    auto prepB = [&]() {
      cs = halves_sum(((__uint_as_float(cg[0].x) + __uint_as_float(cg[1].x)) + (__uint_as_float(cg[2].x) + __uint_as_float(cg[3].x))) + __uint_as_float(cg[4].x));
    };
    auto prepC = [&]() {
      const u32 c11 = pk(cs, cs);
      const float c2 = cs - bflo(c11);
      ca = u32x4{hh ? 0u : c11, hh ? 0u : pk(c2, c2), 0u, 0u};
      const float qs = cb < 5 ? 0.22360679774997896f * 1.4426950408889634f : 1.0f;
      rs[0] = __uint_as_float(xs[0].z) * qs; rs[1] = __uint_as_float(xs[1].z) * qs;
      xs[0] = u32x4{xs[0].x, xs[0].x, 0u, 0u}; xs[1] = u32x4{xs[1].x, xs[1].x, 0u, 0u};
    };
    u32 wov = OOB;
    auto round = [&](auto IT) __attribute__((always_inline)) {
      constexpr int it = decltype(IT)::v;
      constexpr int cu = it & 1, pv = cu ^ 1, hi = (it >> 1) & 1;
      const u32 bo = cu * 32768;
      if (it < 10) {
        if (it == 5) VMCNT(8);
        else if (it == 2 || it == 3) VMCNT(5);
        else if (it == 0 || it == 6 || it == 7) VMCNT(6);
        else VMCNT(4);
        if (it < 8 && it != 5) next();
        BAR();
        if (it == 0) STAMP(23);
        SB();
      }
      if (it == 8) {
        wov = *(LDS u32*)(size_t)chka;
        xs[0] = lds128(xsa); xs[1] = lds128(xsa + 512);
#pragma unroll
        for (int k = 0; k < 5; ++k) cg[k] = *(LDS u32x2*)(size_t)(csa + k * 1024);
      }
      if (it == 10) {
#pragma unroll
        for (int i = 0; i < 4; ++i) acc0 = mfma32(fw[pv][i], fx0[pv][i], acc0);
#pragma unroll
        for (int i = 0; i < 4; ++i) acc1 = mfma32(fw[pv][i], fx1[pv][i], acc1);
        return;
      }
#pragma unroll
      for (int i = 0; i < 4; ++i) {
        if (it > 0) acc0 = mfma32(fw[pv][i], fx0[pv][i], acc0);
        SB();
        fw[cu][i] = ldstr(aw[hi][0] + bo + i * 4096, aw[hi][1] + bo + i * 4096);
        SB();
        if (it > 0) acc1 = mfma32(fw[pv][i], fx1[pv][i], acc1);
        SB();
        fx0[cu][i] = lds128(ax[hi][i] + bo); fx1[cu][i] = lds128(ax[hi][i] + bo + 4096);
        SB();
        if (it == 1) cs4 = mfma16(sel, fw[pv][i], cs4);
        if (it == 5 && i == 0) { glds_dev(rws, fv, fso, fl); glds_dev(rws, fv2, fso + 1024, fl + 1024); }
        if (it == 4) { if (i == 2) next(); piece(2 + (i >> 1), (2 * i) & 3); piece(2 + (i >> 1), (2 * i + 1) & 3); }
        else if (it < 8 && it != 5) piece((it + 2) & 3, i);
        if (it >= 8 && i == 1) glds4(rws, wov, OFF_WO + cb3 * 102400 + wo1 + (it - 8) * 4096, L_WO + wo1 + (it - 8) * 4096);
        if (it == 8) {
          if (i == 0) { woready = __all(wov == tag); wov = woready ? v16 : OOB; asm volatile("" : "+v"(wov)); }
          if (i == 1) { prepA(); asm volatile("" : "+v"(bad)); }
          if (i == 2) { prepB(); asm volatile("" : "+v"(cs)); }
          if (i == 3) { prepC(); asm volatile("" : "+v"(ca), "+v"(xs[0]), "+v"(xs[1]), "+v"(rs[0]), "+v"(rs[1])); }
        }
        SB();
      }
      if (it == 1) {
        const u32x2 gq = {__float_as_uint(cs4[0] + 0.f), tag};
        asm volatile("buffer_store_dwordx2 %0, %1, %2, %3 offen sc1" ::"v"(gq), "v"(csv), "s"(rws), "s"(cso) : "memory");
        SB();
      }
      if (it == 8) {
        if (__builtin_expect(__any(bad != 0), 0)) {
          u32 n = 0;
#pragma unroll 1
          do {
            __builtin_amdgcn_s_sleep(8);
            xs[0] = ld_dev(rws, l31 * 16, OFF_XS + (128 * rb + 64 * wm) * 16);
            xs[1] = ld_dev(rws, l31 * 16, OFF_XS + (128 * rb + 64 * wm + 32) * 16);
#pragma unroll
            for (int k = 0; k < 5; ++k) cg[k] = __builtin_amdgcn_raw_buffer_load_b64(rws, hh * 5120 + l31 * 8, OFF_CSQ + (cb * 10 + k) * 1024 + wn * 256, 16);
            __builtin_amdgcn_s_waitcnt(0x0F70);
            asm volatile("" ::: "memory");
            prepA();
          } while (__any(bad != 0) && ++n < 65536);
          if (n >= 65536) give_up(rws);
          prepB();
          prepC();
        }
        acc0 = mfma32(ca, xs[0], acc0);
        acc1 = mfma32(ca, xs[1], acc1);
        SB();
      }
    };
    round(IC<0>{}); round(IC<1>{}); round(IC<2>{}); round(IC<3>{}); round(IC<4>{}); round(IC<5>{});
    round(IC<6>{}); round(IC<7>{}); round(IC<8>{}); round(IC<9>{}); round(IC<10>{});
    STAMP(1);
#pragma unroll
    for (int mt = 0; mt < 2; ++mt) {
      u32 d[8];
#pragma unroll
      for (int u = 0; u < 8; ++u) d[u] = pk((mt ? acc1[2 * u] : acc0[2 * u]) * rs[mt], (mt ? acc1[2 * u + 1] : acc0[2 * u + 1]) * rs[mt]);
      st_dev(widen32(d[0], d[1], d[2], d[3]), rws, v16, so1 + mt * GS_QKV);
      st_dev(widen32(d[4], d[5], d[6], d[7]), rws, v16, so1 + mt * GS_QKV + 1024);
    }
    STAMP(2);
  } else {
    const u32 sidx = pr * 4 + (j - 28);
    const rsrc_t rr = mkrsrc(pwo, 640 * 1280);
    side_loads();
    const u32 vo = (lane >> 3) * 1280 + (lane & 7) * 16;
#pragma unroll
    for (int k = 0; k < 2; ++k) {
      const u32 u = sidx * 8 + w + k * 128, kb = (u * 6554) >> 16, nb = u - kb * 10;
#pragma unroll
      for (int i = 0; i < 4; ++i) glds_nt(rr, u < 200 ? vo : OOB, (32 * kb + 8 * i) * 1280 + nb * 128, w * 8192 + k * 4096 + i * 1024);
    }
    if (tid < 32) *(LDS u32*)(size_t)(L_CTRL + tid * 4) = 0;
    VMCNT(8);
    row_stats();
    VMCNT(2);
    const u32 ta = w * 8192 + (8 * g4 + q4) * 128 + 8 * p4;
#pragma unroll
    for (int k = 0; k < 2; ++k) {
      const u32 u = sidx * 8 + w + k * 128, kb = (u * 6554) >> 16, nb = u - kb * 10;
#pragma unroll
      for (int f = 0; f < 4; ++f) {
        const u32 ctile = 4 * nb + f, cbk = (ctile * 205) >> 10, ct = ctile - cbk * 5;
        st_dev(ldstr(ta + k * 4096 + 32 * f, ta + k * 4096 + 32 * f + 512), rws, u < 200 ? v16 : OOB, OFF_WO + ((cbk * 20 + kb) * 5 + ct) * 1024);
      }
    }
    VMCNT(0);
    BAR();
    if (w < 4) post(rws, lane * 128, OFF_MBW + w * 8192 + sidx * 4, tag);
    await(rws, v4, OFF_MBW + b * 128, tag, 0xffffull);
    glds4(rws, v16, OFF_WO + (cb3 * 100 + 8 + 8 * w) * 1024, L_WO + (8 + 8 * w) * 1024);
    glds4(rws, v16, OFF_WO + (cb3 * 100 + 12 + 8 * w) * 1024, L_WO + (12 + 8 * w) * 1024);
    woready = true;
    __builtin_amdgcn_s_sleep(SPARE_SLEEP);
  }
  DRAIN();
  STAMP(3);
  BAR();
  if (w == 0) post(rws, post1, OFF_MB1, tag);
  if (w == 1) set_flag(rws, OFF_SEEN + b * 4, tag);
  u32x2 xr[2][2];
#pragma unroll
  for (int pp = 0; pp < 2; ++pp)
#pragma unroll
    for (int m2 = 0; m2 < 2; ++m2)
      xr[pp][m2] = __builtin_amdgcn_raw_buffer_load_b64(rx, (pp && w4) ? OOB : (16 * m2 + l15) * 1280 + g4 * 8, gr * 40960 + 160 * cb3 + 32 * (pp ? 4 : w4), 0);
  f32x16 c0, c8;
  {
    const int e = (int)l31 - 4 * (int)hh;
#pragma unroll
    for (int r = 0; r < 16; ++r) { const bool cnd = 8 * (r >> 2) + (r & 3) > e; c0[r] = cnd ? 0.f : -1e30f; c8[r] = cnd ? -1e30f : 0.f; }
    asm volatile("" : "+v"(c0), "+v"(c8));
  }
  const u32 ka = opaque(hh * KVS + (32 * w + l31) * 16), kb = opaque((2 + 4 * hh) * KVS + (32 * w + l31) * 16);
  const u32 va = opaque((blk ? 5 + (p4 & 1) : 3 + (p4 >> 1)) * KVS + 8 * (blk ? odd & ~p4 : p4 & 1) + (32 * w + 4 * hh + q4) * 16);
  const u32 vkv = opaque(hh * GS_QKV + l31 * 16), vlo = opaque(hh ? OOB : v16);
  const u32 sq = sopaque(OFF_QKV + (8 * c + w) * GS_QKV), skv = sopaque(OFF_QKV + ((c == 0 && w < 4) ? 2 * w : 8 * c - 8 + 2 * w) * GS_QKV);
  const u32 pA = sopaque(plA * 512), pC = sopaque(plC * 512);
  u64 need1;
  {
    const u32 first = pb + 2 * odd;
    need1 = __ballot(lane < 24 && (c > 0 || (lane & 4)) && (lane >= 8 || (lane & 4)) && (!(lane & 1) || ((first + 2) >> 4) != (first >> 4)));
  }
  const u32 ru = (pr * 4 + (j >> 3)) * 4 + w4, rkb = (ru * 43) >> 7, rct = ru - 3 * rkb, rF = (pr * 8 + (j >> 2)) * 8 + 2 * w4;
  auto relay_load = [&](int f) {
    if (f < 2) {
      glds_nt(mkrsrc(pw1, 640 * 3072), ru < 60 ? (lane >> 2) * 3072 + p4 * 16 : OOB, (2 * rkb + f) * 49152 + cb4 * 192 + rct * 64, L_RELAY + w4 * 4096 + f * 1024);
    } else {
      const u32 F = rF + f - 2, k2 = (F * 205) >> 10;
      glds_nt(mkrsrc(pw2, 1536 * 1280), rF < 240 ? (lane >> 1) * 1280 + (lane & 1) * 16 : OOB, k2 * 40960 + cb3 * 160 + (F - 5 * k2) * 32, L_RELAY + w4 * 4096 + f * 1024);
    }
  };
  *(LDS u32x4*)(size_t)(6 * KVS + tid * 16) = u32x4{0x3f803f80u, 0x3f80u, (c == 0 && tid < 256) ? 0xf149u : 0u, 0u};
  if (w == 0) await(rws, v4, mb1, tag, need1);
  if (__builtin_expect(!woready, 0)) {
    await(rws, v4, OFF_MBW + b * 128, tag, 0xffffull);
    glds4(rws, v16, OFF_WO + (cb3 * 100 + 8 + 8 * w) * 1024, L_WO + (8 + 8 * w) * 1024);
    glds4(rws, v16, OFF_WO + (cb3 * 100 + 12 + 8 * w) * 1024, L_WO + (12 + 8 * w) * 1024);
  }
  BAR();
  STAMP(4);
  u32x4 q1 = ld_raw(rws, v16, sq + pA), q2 = ld_raw(rws, vlo, sq + pC);
  u32x4 k1 = ld_raw(rws, v16, sq + pA + 40960), k2 = ld_raw(rws, vlo, sq + pC + 40960);
  glds_dev(rws, vkv, skv + pA + 40960, w * 1024);
  glds_dev(rws, vkv, skv + pA + 41472, w * 1024 + KVS);
  glds_dev(rws, vkv, skv + pC + 40960, w * 1024 + 2 * KVS);
  glds_dev(rws, vkv, skv + pA + 81920, w * 1024 + 3 * KVS);
  glds_dev(rws, vkv, skv + pA + 82432, w * 1024 + 4 * KVS);
  glds_dev(rws, vkv, skv + pC + 81920, w * 1024 + 5 * KVS);
  if (half) {
    VMCNT(0);
    BAR();
    STAMP(5);
    relay_load(0); relay_load(1); relay_load(2);
  }
  asm volatile("s_waitcnt vmcnt(6)" : "+v"(q1), "+v"(q2), "+v"(k1), "+v"(k2)::"memory");
  if (odd) { q2.x = 0u; q2.y = 0u; } else { q2.z = 0u; q2.w = 0u; }
  float shift;
  {
    float d = dot2(k1.x, q1.x, 0.f), d2 = dot2(k2.x, q2.x, 0.f);
    d = dot2(k1.y, q1.y, d); d2 = dot2(k2.y, q2.y, d2);
    d = dot2(k1.z, q1.z, d); d2 = dot2(k2.z, q2.z, d2);
    d = dot2(k1.w, q1.w, d); d2 = dot2(k2.w, q2.w, d2);
    shift = halves_sum(d + d2);
  }
  q2.z |= hh ? 0x3f80u : 0u;
  k2.x |= hh ? 0x3f803f80u : 0u;
  k2.y |= hh ? 0x3f80u : 0u;
  u32x4 w2f[6][5];
  const u32 w2o = sopaque(OFF_W2 + (cb3 * 48 + 6 * w) * 5120);
  u32x4 ow;
  u32x2 oc;
#pragma unroll 1
  for (int attempt = 0;; ++attempt) {
    u32x4 q3 = q2;
    {
      const float h1 = bflo(pk(-shift, 0.f)), r1 = -shift - h1, h2 = bflo(pk(r1, 0.f));
      q3.x = hh ? pk(h1, h2) : q2.x;
      q3.y = hh ? pk(r1 - h2, 0.f) : q2.y;
    }
    const f32x16 z = {};
    f32x16 sc = mfma32(k1, q1, c8), sn, o = z;
    sc = mfma32(k2, q3, sc);
    if (attempt == 0 && !half) {
      SB();
      VMCNT(0);
      BAR();
      STAMP(5);
      __builtin_amdgcn_s_setprio(1);
    }
    u32x4 ka1 = lds128(ka), ka2 = lds128(kb), vf[2];
#pragma unroll
    for (int i = 0; i < 9; ++i) {
      const int t = i == 0 ? 8 : i - 1;
      SB();
      vf[0] = ldstr(va + t * 512, va + t * 512 + 128);
      vf[1] = ldstr(va + t * 512 + 256, va + t * 512 + 384);
      if (i > 0 && i < 8) {
        sn = mfma32(ka1, q1, z);
        sn = mfma32(ka2, q3, sn);
        if (i < 7) { ka1 = lds128(ka + (i + 1) * 512); ka2 = lds128(kb + (i + 1) * 512); }
      }
      if (half && attempt == 0) {
        if (i == 1) relay_load(3);
        if (i == 3) {
          const u32 f = w4 < 3 ? 72 + 4 * w4 : 0;
          if (w4 == 0) glds4(rws, v16, OFF_WO + (cb3 * 100 + 4) * 1024, L_WO + 4096);
          glds4(rws, v16, OFF_WO + (cb3 * 100 + f) * 1024, L_WO + f * 1024);
        }
        if (i == 4) {
          VMCNT(4);
          const u32 a1 = L_RELAY + w4 * 4096 + (8 * hh + q4) * 64 + blk * 32 + 8 * p4, a2 = L_RELAY + w4 * 4096 + 2048 + (8 * g4 + q4) * 32 + 8 * p4;
          const u32x4 r0 = ldstr(a1, a1 + 256), r1 = ldstr(a1 + 1024, a1 + 1280), r2 = ldstr(a2, a2 + 128), r3 = ldstr(a2 + 1024, a2 + 1152);
          f32x4 t = {};
          t = mfma16(sel, r0, t);
          t = mfma16(sel, r1, t);
          const u32 v1 = ru < 60 ? v16 : OOB, v2 = rF < 240 ? v16 : OOB;
          st_dev(r0, rws, v1, OFF_W1 + ((cb4 * 40 + 2 * rkb) * 3 + rct) * 1024);
          st_dev(r1, rws, v1, OFF_W1 + ((cb4 * 40 + 2 * rkb + 1) * 3 + rct) * 1024);
          st_dev(r2, rws, v2, OFF_W2 + (cb3 * 240 + rF) * 1024);
          st_dev(r3, rws, v2, OFF_W2 + (cb3 * 240 + rF + 1) * 1024);
          const float t0 = t[0] + 0.f;
          asm volatile("buffer_store_dword %0, %1, %2, %3 offen sc1" ::"v"(t0), "v"((ru >= 60 || hh) ? OOB : v4), "s"(rws),
                       "s"(OFF_CSP + rkb * 6144 + (cb4 * 3 + rct) * 128) : "memory");
          glds4(rws, v16, OFF_WO + (cb3 * 100 + 84 + 4 * w4) * 1024, L_RELAY + w4 * 4096);
        }
        if (i == 6) {
          VMCNT(0);
          if (lds_ticket(C_WO) == 3) post(rws, lane < 32 ? ((lane & 7) * 32 + (lane >> 3) * 2) * 128 : OOB, OFF_MB0 + ((j & 3) * 8 + xo) * 128 + (pr * 8 + (j >> 2)) * 4, tag);
        }
      }
      SB();
#pragma unroll
      for (int u = 0; u < 2; ++u) {
        float p[8];
#pragma unroll
        for (int r = 0; r < 8; ++r) p[r] = __builtin_amdgcn_exp2f(sc[8 * u + r]);
        if (i == 0 && u == 0) {
          SB();
          sn = mfma32(ka1, q1, c0);
          sn = mfma32(ka2, q3, sn);
          ka1 = lds128(ka + 512); ka2 = lds128(kb + 512);
          SB();
        }
        o = mfma32(vf[u], u32x4{pk(p[0], p[1]), pk(p[2], p[3]), pk(p[4], p[5]), pk(p[6], p[7])}, o);
      }
      sc = sn;
    }
    SB();
    float lsum;
    { auto r = __builtin_amdgcn_permlane32_swap(__float_as_uint(o[8]), __float_as_uint(o[8]), false, false); lsum = __uint_as_float(r[1]); }
    const float inv = __builtin_amdgcn_rcpf(lsum);
    const f32x2 chk = ((f32x2{o[0], o[1]} + f32x2{o[2], o[3]}) + (f32x2{o[4], o[5]} + f32x2{o[6], o[7]})) + (f32x2{o[8], o[9]} + f32x2{o[10], o[11]});
    const bool ok = lsum <= 1e15f && lsum >= 0.25f && fabsf(chk.x + chk.y) <= 3e38f;
    ow = widen32(pk(o[0] * inv, o[1] * inv), pk(o[2] * inv, o[3] * inv), pk(o[4] * inv, o[5] * inv), pk(o[6] * inv, o[7] * inv));
    oc = u32x2{pk(o[8] * inv, o[9] * inv), pk(o[10] * inv, o[11] * inv)};
    asm volatile("" : "+v"(ow), "+v"(oc));
    if (__builtin_expect(__all(ok) || attempt, 1)) break;
    float m = -3e38f;
#pragma unroll 1
    for (int t = 0; t < 9; ++t) {
      f32x16 sv = mfma32(lds128(ka + t * 512), q1, z);
      sv = mfma32(lds128(kb + t * 512), q2, sv);
#pragma unroll
      for (int r = 0; r < 16; ++r) m = fmaxf(m, sv[r] + (t == 0 ? c0[r] : t == 8 ? c8[r] : 0.f));
    }
    auto r = __builtin_amdgcn_permlane32_swap(__float_as_uint(m), __float_as_uint(m), false, false);
    shift = fmaxf(__uint_as_float(r[0]), __uint_as_float(r[1]));
  }
  st_dev(ow, rws, v16, OFF_O + (8 * c + w) * GS_O + pA);
  __builtin_amdgcn_raw_buffer_store_b64(oc, rws, vlo, OFF_O + (8 * c + w) * GS_O + pC + 8 * odd, 16);
  STAMP(6);

  const u32 vq = opaque(g4 * 512 + l15 * 16);
  const u32 s0l = opaque(L_S0 + v16);
  f32x4 acc[2][5] = {};
  {
    const u32 pv2 = lane < 8 ? lane * 512 : OOB, ps2 = OFF_MB2 + ((8 * c + w) * 32 + (head >> 3)) * 128 + (head & 7) * 4;
    if (!half) { DRAIN(); post(rws, pv2, ps2, tag); STAMP(7); }
    u32x4 wf[5][5];
    lds_wait(C_WO, 4);
#pragma unroll
    for (int cc = 0; cc < 5; ++cc) {
      const u32 a = opaque(L_WO + w4 * 25600 + (cc < 4 ? ((w4 + cc) & 3) : 4) * 1024 + v16);
#pragma unroll
      for (int si = 0; si < 5; ++si) wf[si][cc] = lds128(a + si * 5120);
    }
    SB();
    lds_inc(C_FREE);
    if (half) { DRAIN(); post(rws, pv2, ps2, tag); STAMP(7); }
    if (b == 0 && w == 3) {
      u32 n = 0;
#pragma unroll 1
      for (;;) {
        const u32x4 f = ld_dev(rws, v16, OFF_SEEN);
        asm volatile("" ::: "memory");
        if (__all(f.x == tag && f.y == tag && f.z == tag && f.w == tag) || ++n > 16384) break;
        __builtin_amdgcn_s_sleep(32);
      }
      set_flag(rws, OFF_EPOCH, tag);
    }
#pragma unroll
    for (int m2 = 0; m2 < 2; ++m2) {
      acc[m2][0] = f32x4{bflo(xr[0][m2].x), bfhi(xr[0][m2].x), bflo(xr[0][m2].y), bfhi(xr[0][m2].y)};
      acc[m2][4] = f32x4{bflo(xr[1][m2].x), bfhi(xr[1][m2].x), bflo(xr[1][m2].y), bfhi(xr[1][m2].y)};
      asm volatile("" : "+v"(acc[m2][0]), "+v"(acc[m2][4]));
    }
    const u32 so = sopaque(OFF_O + gr * GS_O + w4 * 10240);
    if (half) {
      await(rws, v4, OFF_MB0 + b * 128, tag, 0xffffffffull);
      lds_wait(C_FREE, 8);
#pragma unroll
      for (int i = 0; i < 2; ++i) glds4(rws, v16, OFF_W1 + cb4 * 122880 + (i * 60 + 4 * w4) * 1024, (i * 60 + 4 * w4) * 1024);
    }
    await(rws, v4, mb2, tag, 0xffull);
    STAMP(8);
    u32x4 af[5][2];
#pragma unroll
    for (int n = 0; n < 10; ++n) af[n >> 1][n & 1] = ld_at(rws, vq, so, (n >> 1) * 2048 + (n & 1) * 256);
    SB();
    lds_wait(half ? C_SCR : C_FREE, half ? 4 : 8);
#pragma unroll
    for (int si = 0; si < 5; ++si)
#pragma unroll
      for (int m2 = 0; m2 < 2; ++m2)
#pragma unroll
        for (int cc = 0; cc < 5; ++cc) acc[m2][cc] = mfma16(wf[si][cc], af[si][m2], acc[m2][cc]);
  }
  STAMP(9);
  if (half) {
    SB();
    glds4(rws, v16, OFF_W1 + cb4 * 122880 + ((w4 >> 1) * 60 + 16 + 4 * (w4 & 1)) * 1024, ((w4 >> 1) * 60 + 16 + 4 * (w4 & 1)) * 1024);
  }
#pragma unroll
  for (int cc = 1; cc < 5; ++cc) {
    const u32 a = s0l + (cc < 4 ? (((w4 + cc) & 3) * 3 + cc - 1) * 2048 : 24576 + w4 * 2048);
    lds_st4(a, acc[0][cc]);
    lds_st4(a + 1024, acc[1][cc]);
  }
  lds_inc(C_X3 + half * 4);
  lds_wait(C_X3 + half * 4, 4);
  u32 hp[2][2][2];
#pragma unroll
  for (int pp = 0; pp < 2; ++pp) {
    if (pp == 0 || w4 == 0) {
      const u32 a = pp ? s0l + 24576 + 2048 : s0l + w4 * 6144;
#pragma unroll
      for (int m2 = 0; m2 < 2; ++m2) {
        const f32x4 v = (acc[m2][pp ? 4 : 0] + ldsf4(a + m2 * 1024)) + (ldsf4(a + 2048 + m2 * 1024) + ldsf4(a + 4096 + m2 * 1024));
        hp[pp][m2][0] = pk(v.x, v.y);
        hp[pp][m2][1] = pk(v.z, v.w);
      }
      st_dev(widen16(hp[pp][0][0], hp[pp][0][1], hp[pp][1][0], hp[pp][1][1]), rws, v16, OFF_H + gr * GS_O + (5 * cb3 + (pp ? 4 : w4)) * 1024);
    } else {
      hp[pp][0][0] = hp[pp][0][1] = hp[pp][1][0] = hp[pp][1][1] = 0u;
    }
  }
  lds_inc(C_SCR);
  STAMP(10);
  {
    float sv[2], qv[2];
#pragma unroll
    for (int m2 = 0; m2 < 2; ++m2) {
      sv[m2] = 0.f; qv[m2] = 0.f;
#pragma unroll
      for (int pp = 0; pp < 2; ++pp)
#pragma unroll
        for (int k = 0; k < 2; ++k) { sv[m2] = dot2(hp[pp][m2][k], 0x3f803f80u, sv[m2]); qv[m2] = dot2(hp[pp][m2][k], hp[pp][m2][k], qv[m2]); }
    }
    auto r1 = __builtin_amdgcn_permlane16_swap(__float_as_uint(sv[0]), __float_as_uint(sv[1]), false, false);
    auto r2 = __builtin_amdgcn_permlane16_swap(__float_as_uint(qv[0]), __float_as_uint(qv[1]), false, false);
    auto r3_ = __builtin_amdgcn_permlane32_swap(__float_as_uint(__uint_as_float(r1[0]) + __uint_as_float(r1[1])),
                                                __float_as_uint(__uint_as_float(r2[0]) + __uint_as_float(r2[1])), false, false);
    *(LDS float*)(size_t)(L_HS + w * 256 + lane * 4) = __uint_as_float(r3_[0]) + __uint_as_float(r3_[1]);
  }
  DRAIN();
  if (half) lds_inc(C_W1);
  if (lds_ticket(C_HS + half * 4) == 3) {
    post(rws, lane < 16 ? lane * 256 : OOB, OFF_MB3 + (gr * 32 + (cb3 >> 2)) * 128 + (cb3 & 3) * 4, tag);
    const u32 a = opaque(L_HS + half * 1024 + lane * 4);
    const float t = (*(LDS float*)(size_t)a + *(LDS float*)(size_t)(a + 256)) + (*(LDS float*)(size_t)(a + 512) + *(LDS float*)(size_t)(a + 768));
    __builtin_amdgcn_raw_buffer_store_b64(u32x2{__float_as_uint(t), tag}, rws, lane * 8, OFF_HS + (gr * 8 + cb3) * 512, 16);
  }
  STAMP(11);
  if (!half) {
    await(rws, v4, OFF_MB0 + b * 128, tag, 0xffffffffull);
#pragma unroll
    for (int i = 0; i < 6; ++i) glds3(rws, v16, OFF_W1 + cb4 * 122880 + ((i / 3) * 60 + 24 + 9 * w4 + 3 * (i % 3)) * 1024, ((i / 3) * 60 + 24 + 9 * w4 + 3 * (i % 3)) * 1024);
    if (w < 3) {
      const u32 col = cb4 * 96 + w * 32;
      float t[10];
#pragma unroll
      for (int k = 0; k < 10; ++k) t[k] = __uint_as_float(__builtin_amdgcn_raw_buffer_load_b32(rws, hh * 61440 + l31 * 4, OFF_CSP + col * 4 + k * 6144, 16));
      const u32 bv = __builtin_amdgcn_raw_buffer_load_b16(mkrsrc(pb1, 3072), l31 * 2, col * 2, 0);
      const float cs = halves_sum(((t[0] + t[1]) + (t[2] + t[3])) + ((t[4] + t[5]) + (t[6] + t[7])) + (t[8] + t[9]));
      const u32 c11 = pk(cs, cs);
      const float c2 = cs - bflo(c11);
      *(LDS u32x4*)(size_t)(L_AUG + w * 512 + l31 * 16) = u32x4{c11, pk(c2, c2), bv * 0x10001u, 0u};
    }
  }

  {
    const u32 so = sopaque(OFF_H + g4r * GS_O + kh * 20480);
    if (!half) { VMCNT(0); lds_inc(C_W1); }
    await(rws, v4, mb3, tag, 0xfull);
    STAMP(12);
    u32x4 hf[4][5];
#pragma unroll
    for (int n = 0; n < 8; ++n) hf[n / 5][n % 5] = ld_at(rws, v16, so, n * 1024);
    u32x2 hs[8];
    const u32 hso = OFF_HS + g4r * 4096, v8 = opaque(lane * 8);
    auto load_hs = [&]() {
#pragma unroll
      for (int k = 0; k < 8; ++k) hs[k] = __builtin_amdgcn_raw_buffer_load_b64(rws, v8 + k * 512, hso, 16);
    };
    lds_wait(C_W1, 8);
    STAMP(15);
    f32x16 a4[3] = {};
    const u32 wa = kh * 61440 + (lane ^ (kh << 4)) * 16, wx = opaque(L_AUG + (l31 ^ (kh << 4)) * 16);
    u32x4 ring[6];
#pragma unroll
    for (int n = 0; n < 6; ++n) ring[n] = lds128(wa + n * 1024);
    float rs;
    u32 tp, bad;
    auto stats = [&]() {
      bad = 0;
#pragma unroll
      for (int k = 0; k < 8; ++k) bad |= hs[k].y ^ tag;
      const float tot = ((__uint_as_float(hs[0].x) + __uint_as_float(hs[1].x)) + (__uint_as_float(hs[2].x) + __uint_as_float(hs[3].x))) +
                        ((__uint_as_float(hs[4].x) + __uint_as_float(hs[5].x)) + (__uint_as_float(hs[6].x) + __uint_as_float(hs[7].x)));
      const auto rt = __builtin_amdgcn_permlane32_swap(__float_as_uint(tot), __float_as_uint(tot), false, false);
      const float mu = __uint_as_float(rt[0]) * (1.0f / 640.0f);
      const float var = fmaxf(__uint_as_float(rt[1]) * (1.0f / 640.0f) - mu * mu, 0.f);
      const float v1 = var + 1e-5f;
      const float d2 = v1 * (var * __builtin_amdgcn_rcpf(v1) + 1e-5f);
      rs = __builtin_amdgcn_rsqf(d2);
      const float tv = kh ? d2 * rs : -mu;
      const float t1 = bflo(pk(tv, 0.f));
      tp = hh ? 0u : pk(t1, tv - t1);
    };
#pragma unroll
    for (int cc = 0; cc < 4; ++cc) {
      if (cc == 3) stats();
#pragma unroll
      for (int n = 15 * cc; n < 15 * cc + 15; ++n) {
        if (cc < 3) SB();
        a4[n % 3] = mfma32(ring[n % 6], hf[cc][(n % 15) / 3], a4[n % 3]);
        if (n + 6 < 60) ring[n % 6] = lds128(wa + (n + 6) * 1024);
        else if (n + 6 < 63) ring[n % 6] = lds128(wx + (n + 6 - 60) * 512);
        if (n % 2 == 0 && n / 2 + 8 < 20) hf[(n / 2 + 8) / 5][(n / 2 + 8) % 5] = ld_at(rws, v16, so, (n / 2 + 8) * 1024);
        if (n >= 24 && n < 32) hs[n - 24] = __builtin_amdgcn_raw_buffer_load_b64(rws, v8 + (n - 24) * 512, hso, 16);
        if (n >= 33 && n % 3 == 0) w2f[(n / 3 - 11) / 5][(n / 3 - 11) % 5] = ld_at(rws, v16, w2o, (n / 3 - 11) * 1024);
      }
      if (cc == 3) {
#pragma unroll
        for (int n = 0; n < 15; ++n) {
          __builtin_amdgcn_sched_group_barrier(0x8, 1, 0);
          __builtin_amdgcn_sched_group_barrier(0x100, 1, 0);
          if (n % 3 == 0) __builtin_amdgcn_sched_group_barrier(0x20, 1, 0);
          __builtin_amdgcn_sched_group_barrier(0x402, 4, 0);
        }
      }
      SB();
      if (cc == 2) { lds_wait(C_SCR, 8 + 4 * half); SB(); }
    }
    if (__builtin_expect(__any(bad != 0), 0)) {
      u32 it = 0;
#pragma unroll 1
      do {
        load_hs();
        asm volatile("" ::: "memory");
        stats();
      } while (__any(bad != 0) && ++it < 4096);
      if (it >= 4096) give_up(rws);
    }
    {
      const u32x4 bx = {kh ? 0u : tp, kh ? 0u : tp, kh ? tp : 0u, 0u};
#pragma unroll
      for (int ct = 0; ct < 3; ++ct) a4[ct] = mfma32(ring[ct], bx, a4[ct]);
    }
    STAMP(13);
    lds_inc(C_FREE2);
    {
      const u32 a = s0l + w4 * 6144;
#pragma unroll
      for (int ct = 0; ct < 3; ++ct) {
        lds_st4(a + ct * 2048, f32x4{a4[ct][8], a4[ct][9], a4[ct][10], a4[ct][11]});
        lds_st4(a + ct * 2048 + 1024, f32x4{a4[ct][12], a4[ct][13], a4[ct][14], a4[ct][15]});
      }
    }
    lds_inc(C_X4 + (w >> 1) * 4);
#pragma unroll
    for (int n = 9; n < 14; ++n) w2f[n / 5][n % 5] = ld_at(rws, v16, w2o, n * 1024);
    lds_wait(C_X4 + (w >> 1) * 4, 2);
    STAMP(18);
    const u32 oa = s0l + (w4 ^ 1) * 6144;
    f32x4 ot[6];
#pragma unroll
    for (int k = 0; k < 6; ++k) ot[k] = ldsf4(oa + k * 1024);
    SB();
    lds_inc(C_SCR);
#pragma unroll
    for (int ct = 0; ct < 3; ++ct) {
      u32 d[4];
#pragma unroll
      for (int g = 0; g < 4; ++g) {
        if (ct < 2) {
          const int n = 14 + 8 * ct + 2 * g;
          w2f[n / 5][n % 5] = ld_at(rws, v16, w2o, n * 1024);
          w2f[(n + 1) / 5][(n + 1) % 5] = ld_at(rws, v16, w2o, (n + 1) * 1024);
        }
        const f32x2 u = (f32x2{a4[ct][2 * g], a4[ct][2 * g + 1]} + f32x2{ot[2 * ct + (g >> 1)][(2 * g) & 3], ot[2 * ct + (g >> 1)][(2 * g + 1) & 3]}) * rs;
        const f32x2 z = u * (u * u * -0.1029432397f + -2.302208198f);
        const f32x2 dn = f32x2{__builtin_amdgcn_exp2f(z.x), __builtin_amdgcn_exp2f(z.y)} + 1.0f;
        const f32x2 y = u * f32x2{__builtin_amdgcn_rcpf(dn.x), __builtin_amdgcn_rcpf(dn.y)};
        d[g] = pk(y.x, y.y);
      }
      st_dev(widen32(d[0], d[1], d[2], d[3]), rws, v16, OFF_H1 + g4r * GS_H1 + (6 * cb4 + 2 * ct + kh) * 1024);
    }
    STAMP(14);
    if (half) await(rws, v4, mb4, tag, 0xfull);
    DRAIN();
    post(rws, lane < 8 ? lane * 1024 : OOB, OFF_MB4 + (g4r * 64 + (cb4 >> 1)) * 128 + ((cb4 & 1) * 2 + kh) * 4, tag);
  }
  u32x2 b2r[2];
#pragma unroll
  for (int pp = 0; pp < 2; ++pp) b2r[pp] = __builtin_amdgcn_raw_buffer_load_b64(mkrsrc(pb2, 1280), 8 * g4, 160 * cb3 + 32 * (pp ? 4 : w4), 0);
  __builtin_amdgcn_s_setprio(0);

  const rsrc_t ro = mkrsrc(pout, 2048 * 1280);
  u32x4 af[12];
  const u32 so5 = sopaque(OFF_H1 + g3 * GS_H1 + w * 12288);
  if (!half) await(rws, v4, mb4, tag, 0xfull);
  STAMP(16);
#pragma unroll
  for (int n = 0; n < 6; ++n) af[n] = ld_at(rws, vq, so5, (n >> 1) * 2048 + (n & 1) * 256);
  SB();
#pragma unroll
  for (int X = 0; X < 2; ++X) {
    f32x4 a5[2][5] = {};
#pragma unroll
    for (int n = 0; n < 12; ++n) {
#pragma unroll
      for (int ct = 0; ct < 5; ++ct) a5[n & 1][ct] = mfma16(w2f[n >> 1][ct], af[n], a5[n & 1][ct]);
      if (n < 6) af[n + 6] = ld_at(rws, vq, so5 + X * 4 * GS_H1, ((n + 6) >> 1) * 2048 + (n & 1) * 256);
      SB();
    }
    if (X == 0) lds_wait(C_FREE2, 8);
    if (X == 0) STAMP(17); else STAMP(20);
    {
      const u32 a0 = opaque(X * 81920 + w * 1024 + v16), a1 = opaque(X * 81920 + 49152 + w * 1024 + v16);
#pragma unroll
      for (int ct = 0; ct < 5; ++ct)
#pragma unroll
        for (int m2 = 0; m2 < 2; ++m2) lds_st4(ct < 3 ? a0 + (ct * 2 + m2) * 8192 : a1 + (ct * 2 + m2 - 6) * 8192, a5[m2][ct]);
    }
    if (X == 0) {
      await(rws, v4, mb4 + 32768, tag, 0xfull);
#pragma unroll
      for (int n = 0; n < 6; ++n) af[n] = ld_at(rws, vq, so5 + 4 * GS_H1, (n >> 1) * 2048 + (n & 1) * 256);
      SB();
    }
    BAR();
    if (X == 0) STAMP(19); else STAMP(21);
    if (half == X) {
#pragma unroll
      for (int pp = 0; pp < 2; ++pp) {
        if (pp == 0 || w4 == 0) {
          const u32 ct = pp ? 4 : w4;
          const u32 a = opaque(X * 81920 + ct * 16384 + v16);
          u32 d[2][2];
#pragma unroll
          for (int m2 = 0; m2 < 2; ++m2) {
            f32x4 t[8];
#pragma unroll
            for (int k = 0; k < 8; ++k) t[k] = ldsf4(a + m2 * 8192 + k * 1024);
            f32x4 v = {bflo(hp[pp][m2][0]) + bflo(b2r[pp].x), bfhi(hp[pp][m2][0]) + bfhi(b2r[pp].x), bflo(hp[pp][m2][1]) + bflo(b2r[pp].y),
                       bfhi(hp[pp][m2][1]) + bfhi(b2r[pp].y)};
            v += ((t[0] + t[1]) + (t[2] + t[3])) + ((t[4] + t[5]) + (t[6] + t[7]));
            d[m2][0] = pk(v.x, v.y);
            d[m2][1] = pk(v.z, v.w);
          }
          st_plain(widen16(d[0][0], d[0][1], d[1][0], d[1][1]), ro, l31 * 1280 + hh * 16, gr * 40960 + (80 * cb3 + 16 * ct) * 2);
        }
      }
    }
  }
}

extern "C" void kernel_launch(void* const* d_in, const int* in_sizes, int n_in, void* d_out, int out_size, void* d_ws, size_t ws_size,
                              hipStream_t stream) {
  fused_block<<<256, 512, 0, stream>>>(d_in[0], d_in[1], d_in[2], d_in[4], d_in[5], d_in[6], d_in[7], d_out, d_ws);
}
```

```cpp
#include <hip/hip_runtime.h>

typedef unsigned int u32;
typedef unsigned long long u64;
typedef __bf16 bf16x2 __attribute__((ext_vector_type(2)));
typedef __bf16 bf16x8 __attribute__((ext_vector_type(8)));
typedef short s16x4 __attribute__((ext_vector_type(4)));
typedef float f32x2 __attribute__((ext_vector_type(2)));
typedef float f32x4 __attribute__((ext_vector_type(4)));
typedef float f32x16 __attribute__((ext_vector_type(16)));
typedef u32 u32x2 __attribute__((ext_vector_type(2)));
typedef u32 u32x4 __attribute__((ext_vector_type(4)));
typedef __amdgpu_buffer_rsrc_t rsrc_t;
#define LDS __attribute__((address_space(3)))
#define DI __device__ __forceinline__

constexpr u32 GS_QKV = 240 * 512, GS_O = 80 * 512, GS_H1 = 192 * 512;
constexpr u32 OFF_EPOCH = 0, OFF_ERR = 128, OFF_SEEN = 1024, OFF_XS = 16384, OFF_CSQ = 65536;
constexpr u32 OFF_MB1 = OFF_CSQ + 160 * 1024, OFF_MB2 = OFF_MB1 + 256 * 128, OFF_MB3 = OFF_MB2 + 2048 * 128, OFF_MB4 = OFF_MB3 + 2048 * 128;
constexpr u32 OFF_MB0 = OFF_MB4 + 4096 * 128, OFF_MBW = OFF_MB0 + 256 * 128, OFF_HS = OFF_MBW + 272 * 128;
constexpr u32 OFF_QKV = OFF_HS + 512 * 512, OFF_O = OFF_QKV + 64 * GS_QKV, OFF_H = OFF_O + 64 * GS_O, OFF_H1 = OFF_H + 64 * GS_O;
constexpr u32 OFF_WO = OFF_H1 + 64 * GS_H1, OFF_W1 = OFF_WO + 800 * 1024, OFF_W2 = OFF_W1 + 1920 * 1024;
constexpr u32 OFF_CSP = OFF_W2 + 1920 * 1024;
constexpr u32 WS_END = OFF_CSP + 20 * 6144;
#define OFF_ERR_DEF OFF_ERR
#define HAVE_PLANES 2
#define STAMP(i)

constexpr u32 L_XROW = 147456, L_JUNK = 162816;
constexpr u32 KVS = 8256, L_WO = 57856, L_RELAY = L_WO + 84 * 1024;
constexpr u32 L_S0 = 122880, L_HS = 155648, L_AUG = 160256;
constexpr u32 L_CTRL = 163584;
constexpr u32 C_FREE = L_CTRL, C_X3 = L_CTRL + 4, C_SCR = L_CTRL + 12, C_W1 = L_CTRL + 16, C_X4 = L_CTRL + 20, C_FREE2 = L_CTRL + 36, C_HS = L_CTRL + 40, C_WO = L_CTRL + 48;
#define DELAY4 4
#define SPARE_SLEEP 40
#define PSLEEP 6
#define RSLEEP 4
constexpr u32 CODE_BYTES = 32768;

template <int N> struct IC { static constexpr int v = N; };
DI rsrc_t mkrsrc(const void* p, u32 bytes) { return __builtin_amdgcn_make_buffer_rsrc((void*)p, (short)0, (int)bytes, 0x00020000); }
DI void glds(rsrc_t rs, u32 voff, u32 soff, u32 l) {
  asm volatile("s_mov_b32 m0, %0\n\ts_nop 0\n\tbuffer_load_dwordx4 %1, %2, %3 offen lds" ::"s"(l), "v"(voff), "s"(rs), "s"(soff) : "memory");
}
DI void glds_nt(rsrc_t rs, u32 voff, u32 soff, u32 l) {
  asm volatile("s_mov_b32 m0, %0\n\ts_nop 0\n\tbuffer_load_dwordx4 %1, %2, %3 offen nt lds" ::"s"(l), "v"(voff), "s"(rs), "s"(soff) : "memory");
}
DI void glds_dev(rsrc_t rs, u32 voff, u32 soff, u32 l) {
  asm volatile("s_mov_b32 m0, %0\n\ts_nop 0\n\tbuffer_load_dwordx4 %1, %2, %3 offen sc1 lds" ::"s"(l), "v"(voff), "s"(rs), "s"(soff) : "memory");
}
DI void glds4(rsrc_t rs, u32 voff, u32 soff, u32 l) {
  asm volatile("s_mov_b32 m0, %0\n\ts_nop 0\n\tbuffer_load_dwordx4 %1, %2, %3 offen sc1 lds\n\tbuffer_load_dwordx4 %1, %2, %3 offen offset:1024 sc1 lds\n\t"
               "buffer_load_dwordx4 %1, %2, %3 offen offset:2048 sc1 lds\n\tbuffer_load_dwordx4 %1, %2, %3 offen offset:3072 sc1 lds" ::"s"(l), "v"(voff), "s"(rs), "s"(soff));
}
DI void glds3(rsrc_t rs, u32 voff, u32 soff, u32 l) {
  asm volatile("s_mov_b32 m0, %0\n\ts_nop 0\n\tbuffer_load_dwordx4 %1, %2, %3 offen sc1 lds\n\tbuffer_load_dwordx4 %1, %2, %3 offen offset:1024 sc1 lds\n\t"
               "buffer_load_dwordx4 %1, %2, %3 offen offset:2048 sc1 lds" ::"s"(l), "v"(voff), "s"(rs), "s"(soff));
}
DI void touch(rsrc_t rs, u32 voff, u32 soff) {
  asm volatile("s_mov_b32 m0, %0\n\ts_nop 0\n\tbuffer_load_dword %1, %2, %3 offen sc1 lds" ::"s"(L_JUNK), "v"(voff), "s"(rs), "s"(soff));
}
DI u32x4 ld_dev(rsrc_t rs, u32 voff, u32 soff) { return __builtin_amdgcn_raw_buffer_load_b128(rs, voff, soff, 16); }
DI u32x4 ld_at(rsrc_t rs, u32 voff, u32 soff, u32 c) { return ld_dev(rs, voff + (c & 4095), soff + (c & ~4095u)); }
DI u32x4 ld_raw(rsrc_t rs, u32 voff, u32 soff) {
  u32x4 r;
  asm volatile("buffer_load_dwordx4 %0, %1, %2, %3 offen sc1" : "=&v"(r) : "v"(voff), "s"(rs), "s"(soff) : "memory");
  return r;
}
DI void st_dev(u32x4 v, rsrc_t rs, u32 voff, u32 soff) {
  asm volatile("buffer_store_dwordx4 %0, %1, %2, %3 offen sc1\n\ts_nop 1" ::"v"(v), "v"(voff), "s"(rs), "s"(soff) : "memory");
}
DI void st_plain(u32x4 v, rsrc_t rs, u32 voff, u32 soff) {
  asm volatile("buffer_store_dwordx4 %0, %1, %2, %3 offen\n\ts_nop 1" ::"v"(v), "v"(voff), "s"(rs), "s"(soff) : "memory");
}
DI u32x4 lds128(u32 a) { return *(LDS u32x4*)(size_t)a; }
DI f32x4 ldsf4(u32 a) { return *(LDS f32x4*)(size_t)a; }
DI void lds_st4(u32 a, f32x4 v) { *(LDS f32x4*)(size_t)a = v; }
DI u32x4 ldstr(u32 a0, u32 a1) {
  u32x2 lo = __builtin_bit_cast(u32x2, __builtin_amdgcn_ds_read_tr16_b64_v4i16((LDS s16x4*)(size_t)a0));
  u32x2 hi = __builtin_bit_cast(u32x2, __builtin_amdgcn_ds_read_tr16_b64_v4i16((LDS s16x4*)(size_t)a1));
  return u32x4{lo.x, lo.y, hi.x, hi.y};
}
DI f32x16 mfma32(u32x4 a, u32x4 b, f32x16 c) { return __builtin_amdgcn_mfma_f32_32x32x16_bf16(__builtin_bit_cast(bf16x8, a), __builtin_bit_cast(bf16x8, b), c, 0, 0, 0); }
DI f32x4 mfma16(u32x4 a, u32x4 b, f32x4 c) { return __builtin_amdgcn_mfma_f32_16x16x32_bf16(__builtin_bit_cast(bf16x8, a), __builtin_bit_cast(bf16x8, b), c, 0, 0, 0); }
DI float dot2(u32 a, u32 b, float c) { return __builtin_amdgcn_fdot2_f32_bf16(__builtin_bit_cast(bf16x2, a), __builtin_bit_cast(bf16x2, b), c, false); }
DI float sum8(u32x4 v, float c) { c = dot2(v.x, 0x3f803f80u, c); c = dot2(v.y, 0x3f803f80u, c); c = dot2(v.z, 0x3f803f80u, c); return dot2(v.w, 0x3f803f80u, c); }
DI u32 pk(float a, float b) { f32x2 v = {a, b}; return __builtin_bit_cast(u32, __builtin_convertvector(v, bf16x2)); }
DI float bflo(u32 v) { return __uint_as_float(v << 16); }
DI float bfhi(u32 v) { return __uint_as_float(v & 0xffff0000u); }
DI float halves_sum(float x) { auto r = __builtin_amdgcn_permlane32_swap(__float_as_uint(x), __float_as_uint(x), false, false); return __uint_as_float(r[0]) + __uint_as_float(r[1]); }
DI u32x4 widen32(u32 a0, u32 a1, u32 b0, u32 b1) {
  auto r0 = __builtin_amdgcn_permlane32_swap(a0, b0, false, false);
  auto r1 = __builtin_amdgcn_permlane32_swap(a1, b1, false, false);
  return u32x4{r0[0], r1[0], r0[1], r1[1]};
}
DI u32x4 widen16(u32 a0, u32 a1, u32 b0, u32 b1) {
  auto r0 = __builtin_amdgcn_permlane16_swap(a0, b0, false, false);
  auto r1 = __builtin_amdgcn_permlane16_swap(a1, b1, false, false);
  return u32x4{r0[0], r1[0], r0[1], r1[1]};
}
#define VMCNT(n) asm volatile("s_waitcnt vmcnt(" #n ")" ::: "memory")
#define SB() __builtin_amdgcn_sched_barrier(0)
#define OOB 0x7ffffff0u
#define BAR() do { asm volatile("s_waitcnt lgkmcnt(0)" ::: "memory"); __builtin_amdgcn_s_barrier(); asm volatile("" ::: "memory"); } while (0)
#define DRAIN() do { __builtin_amdgcn_s_waitcnt(0x0F70); asm volatile("s_waitcnt vmcnt(0)" ::: "memory"); } while (0)
DI u32 opaque(u32 v) { asm volatile("" : "+v"(v)); return v; }
DI u32 sopaque(u32 v) { asm volatile("" : "+s"(v)); return v; }
DI void lds_inc(u32 a) { asm volatile("s_mov_b64 exec, 1\n\tds_add_u32 %0, %1\n\ts_mov_b64 exec, -1" ::"v"(a), "v"(1u) : "memory"); }
DI u32 lds_ticket(u32 a) {
  u32 r;
  asm volatile("s_mov_b64 exec, 1\n\tds_add_rtn_u32 %0, %1, %2\n\ts_mov_b64 exec, -1\n\ts_waitcnt lgkmcnt(0)" : "=&v"(r) : "v"(a), "v"(1u) : "memory");
  return __builtin_amdgcn_readfirstlane(r);
}
DI void set_flag(rsrc_t rws, u32 off, u32 tag) {
  asm volatile("s_mov_b64 exec, 1\n\tbuffer_store_dword %0, off, %1, %2 sc1\n\ts_mov_b64 exec, -1" ::"v"(tag), "s"(rws), "s"(off) : "memory");
}
DI void lds_wait(u32 a, u32 target) {
  u32 t, n;
  asm volatile("s_mov_b32 %1, 0x100000\n"
               "1:\n\tds_read_b32 %0, %2\n\ts_waitcnt lgkmcnt(0)\n\tv_cmp_le_u32 vcc, %3, %0\n\ts_cbranch_vccnz 2f\n\t"
               "s_sub_u32 %1, %1, 1\n\ts_cbranch_scc0 1b\n"
               "2:" : "=&v"(t), "=&s"(n) : "v"(a), "s"(target) : "vcc", "scc", "memory");
}
DI void give_up(rsrc_t rws) { __builtin_amdgcn_raw_buffer_store_b32(0xdeadu, rws, OFF_ERR, 0, 16); }
DI void post(rsrc_t rws, u32 voff, u32 soff, u32 tag) {
  asm volatile("buffer_store_dword %0, %1, %2, %3 offen sc1" ::"v"(tag), "v"(voff), "s"(rws), "s"(soff) : "memory");
}
DI void await(rsrc_t rws, u32 v4, u32 soff, u32 tag, u64 mask) {
  u32 f, n;
  asm volatile("s_mov_b32 %1, 0x10000\n\ts_mov_b64 exec, %6\n"
               "1:\n\tv_mov_b32 %0, 0\n\tbuffer_atomic_or %0, %2, %3, %4 offen sc0 sc1\n\ts_waitcnt vmcnt(0)\n\tv_cmp_ne_u32 vcc, %5, %0\n\ts_cbranch_vccz 2f\n\t"
               "s_sub_u32 %1, %1, 1\n\ts_cbranch_scc0 1b\n"
               "2:\n\ts_mov_b64 exec, -1" : "=&v"(f), "=&s"(n) : "v"(v4), "s"(rws), "s"(soff), "s"(tag), "s"(mask) : "vcc", "scc", "memory");
  __builtin_amdgcn_s_waitcnt(0x0F70);
}
template <int CTRL> DI float dpp(float x) { return __builtin_bit_cast(float, __builtin_amdgcn_mov_dpp(__builtin_bit_cast(int, x), CTRL, 0xf, 0xf, true)); }
DI float wave_sum(float x) {
  x += dpp<0xB1>(x); x += dpp<0x4E>(x); x += dpp<0x141>(x); x += dpp<0x128>(x);
  auto r = __builtin_amdgcn_permlane16_swap(__float_as_uint(x), __float_as_uint(x), false, false);
  return halves_sum(__uint_as_float(r[0]) + __uint_as_float(r[1]));
}

__global__ __launch_bounds__(512, 2) void fused_block(const void* __restrict__ px, const void* __restrict__ pwqkv, const void* __restrict__ pwo,
                                                      const void* __restrict__ pw1, const void* __restrict__ pb1, const void* __restrict__ pw2,
                                                      const void* __restrict__ pb2, void* __restrict__ pout, void* pws) {
  __shared__ __attribute__((aligned(16))) char smem[163840];
  STAMP(0);
  asm volatile("" : "+s"(px), "+s"(pwqkv), "+s"(pwo), "+s"(pw1), "+s"(pb1), "+s"(pw2), "+s"(pb2), "+s"(pout), "+s"(pws));
  u32 tag, epoch;
  asm volatile("s_load_dword %0, %1, 0x0" : "=s"(epoch) : "s"(pws));
  const u32 tid = threadIdx.x, lane = tid & 63;
  const u32 w = __builtin_amdgcn_readfirstlane(tid >> 6);
  const u32 b = blockIdx.x, xcd = b & 7, j = b >> 3, pr = xcd >> 1, xo = xcd & 1;
  const u32 hh = lane >> 5, l31 = lane & 31, l15 = lane & 15, g4 = lane >> 4, q4 = (lane & 15) >> 2, p4 = lane & 3, blk = (lane >> 4) & 1;
  const rsrc_t rx = mkrsrc(px, 2048 * 1280);
  const rsrc_t rws = mkrsrc(pws, WS_END);
  asm volatile("" ::"s"((u32)(size_t)(LDS char*)smem));
  const u32 v16 = lane * 16, v4 = lane * 4;

  const bool spare = xo && j >= 28;
  const u32 cb3 = 4 * xo + (j & 3), g3 = 16 * pr + 8 * (j >> 4) + ((j >> 2) & 3);
  const u32 cb4 = 8 * xo + (j & 7);
  const u32 half = w >> 2, w4 = w & 3, gr = g3 + 4 * half;
  const u32 head = 4 * xcd + (j & 3), c = j >> 2, odd = head & 1;
  const u32 pb = 5 * (head >> 1), plA = pb + 3 * odd, plC = pb + 2;
  const u32 kh = w & 1, g4r = 16 * pr + 8 * (j >> 4) + 4 * half + 2 * ((j >> 3) & 1) + ((w >> 1) & 1);
  const u32 mb1 = OFF_MB1 + (c * 32 + head) * 128, mb2 = OFF_MB2 + ((gr * 8 + cb3) * 4 + w4) * 128, mb3 = OFF_MB3 + ((g4r * 16 + cb4) * 2 + kh) * 128,
            mb4 = OFF_MB4 + ((g3 * 8 + cb3) * 8 + w) * 128;
  const u32 selv = (((l15 >> 2) ^ g4) & 1) ? 0u : 0x3f803f80u;
  const u32x4 sel = {selv, selv, selv, selv};
  bool woready = false;
  u32 post1 = OOB;
  const u32 xrow = 512 * pr + (xo * 32 + j) * 8 + w;
  auto side_loads = [&]() {
    if (w == 7) {
      u64 pc;
      asm volatile("s_getpc_b64 %0" : "=s"(pc));
      const rsrc_t rc = mkrsrc((const void*)(pc & ~127ull), CODE_BYTES);
      asm volatile("s_mov_b32 m0, %0\n\ts_nop 0\n\tbuffer_load_dword %1, %2, 0 offen lds" ::"s"(L_JUNK), "v"(lane < 8 ? (j * 8 + lane) * 128 : OOB), "s"(rc) : "memory");
    }
    glds(rx, v16, xrow * 1280, L_XROW + w * 1280);
    asm volatile("s_mov_b32 m0, %0\n\ts_nop 0\n\tbuffer_load_dword %1, %2, %3 offen lds" ::"s"(L_XROW + w * 1280 + 1024), "v"(v4), "s"(rx), "s"(xrow * 1280 + 1024) : "memory");
  };
  auto row_stats = [&]() {
    asm volatile("s_waitcnt lgkmcnt(0)\n\ts_mul_i32 %0, %1, 0x9e3779b1\n\ts_add_u32 %0, %0, 0x7f4a7c15" : "=&s"(tag) : "s"(epoch) : "scc", "memory");
    float v[10], sum = 0.f;
    const u32 a = opaque(L_XROW + w * 1280 + v4);
#pragma unroll
    for (int k = 0; k < 5; ++k) {
      const u32 d = *(LDS u32*)(size_t)(a + k * 256);
      v[2 * k] = bflo(d); v[2 * k + 1] = bfhi(d);
      sum += v[2 * k] + v[2 * k + 1];
    }
    const float mu = wave_sum(sum) * (1.0f / 640.0f);
    float sq = 0.f;
#pragma unroll
    for (int k = 0; k < 10; ++k) sq += (v[k] - mu) * (v[k] - mu);
    const float rstd = __builtin_amdgcn_rsqf(wave_sum(sq) * (1.0f / 640.0f) + 1e-5f);
    const float m1 = bflo(pk(-mu, 0.f));
    st_dev(u32x4{pk(m1, -mu - m1), tag, __float_as_uint(rstd), tag}, rws, lane == 0 ? 0u : OOB, OFF_XS + xrow * 16);
    u32 wl = OFF_O + (8 * c + w) * GS_O + plC * 512 + p4 * 128;
    asm volatile("v_writelane_b32 %0, %1, 4\n\tv_writelane_b32 %0, %2, 5\n\tv_writelane_b32 %0, %3, 6\n\tv_writelane_b32 %0, %4, 7\n\tv_writelane_b32 %0, %5, 8"
                 : "+v"(wl) : "s"(mb2), "s"(mb3), "s"(mb4), "s"(mb4 + 32768), "s"(w ? OFF_MB0 + b * 128 : mb1));
    asm volatile("s_mov_b64 exec, %3\n\tbuffer_atomic_or %0, %1, %2, 0 offen sc1\n\ts_mov_b64 exec, -1" ::"v"(0u), "v"(wl), "s"(rws), "s"(w < 2 ? 0x1ffull : 0xffull) : "memory");
  };
  if (__builtin_expect(!spare, 1)) {
    const u32 rl = (j * (32 + 5 * xo)) >> 8, cl = j - rl * (8 - xo), cb = 8 * xo + cl, rb = 4 * pr + rl;
    const rsrc_t rw = mkrsrc(pwqkv, 640 * 3840);
    const u32 voffA = (8 * w + (lane >> 3)) * 1280 + (((lane & 7) ^ ((4 * w + g4) & 7)) << 4);
    const u32 voffB = (4 * w + g4) * 3840 + ((l15 ^ ((g4 << 2) | (w & 3))) << 4);
    u32 sk = ((cl * 5) >> 2) + ((rl * 5) >> 1) + pr;
    sk = sk >= 10 ? sk - 10 : sk;
    u32 sa = 0, sb = 0;
    auto next = [&]() { sa = rb * 163840 + sk * 128; sb = cb * 256 + sk * 245760; sk = sk == 9 ? 0 : sk + 1; };
    auto piece = [&](int buf, int i) {
      const u32 l = buf * 32768 + w * 1024;
      if (i == 0) glds(rx, voffA, sa, l);
      if (i == 1) glds(rw, voffB, sb, l + 16384);
      if (i == 2) glds(rx, voffA, sa + 81920, l + 8192);
      if (i == 3) glds(rw, voffB, sb + 122880, l + 24576);
    };
    auto stage = [&](int buf) { next(); piece(buf, 0); piece(buf, 1); piece(buf, 2); piece(buf, 3); };
    const u32 sk0 = sk;
    side_loads();
    stage(0);
    if (tid < 32) *(LDS u32*)(size_t)(L_CTRL + tid * 4) = 0;
    const u32 wm = w >> 2, wn = w & 3;
    const u32 ax0 = (64 * wm + l31) * 128 + ((hh ^ ((l31 >> 1) & 7)) << 4);
    const u32 aw0 = 16384 + (8 * hh + q4) * 256 + (((4 * wn + 2 * blk + (p4 >> 1)) ^ ((q4 << 2) | (2 * hh))) << 4) + 8 * (p4 & 1);
    u32 ax[2][4], aw[2][2];
#pragma unroll
    for (int i = 0; i < 4; ++i) { ax[0][i] = opaque(ax0 ^ (i << 5)); ax[1][i] = opaque((ax0 ^ (i << 5)) + 65536); }
    aw[0][0] = opaque(aw0); aw[0][1] = opaque((aw0 ^ 16) + 1024); aw[1][0] = opaque(aw0 + 65536); aw[1][1] = opaque((aw0 ^ 16) + 1024 + 65536);
    const u32 xsa = opaque(L_XROW + 10240 + (64 * wm + l31) * 16), csa = opaque(L_XROW + hh * 5120 + (32 * wn + l31) * 8), chka = opaque(L_XROW + 12288 + l15 * 4);
    const u32 so1 = sopaque(OFF_QKV + (4 * rb + 2 * wm) * GS_QKV + (16 * cb + 4 * wn) * 512);
    const u32 csv = opaque((wm || hh) ? OOB : lane * 8), cso = sopaque(OFF_CSQ + (cb * 10 + sk0) * 1024 + wn * 256);
    const u32 fso = sopaque(w < 5 ? OFF_CSQ + cb * 10240 + w * 2048 : w == 5 ? OFF_XS + rb * 2048 : OFF_MBW + b * 128);
    const u32 fv = opaque((w < 6 || lane < 8) ? v16 : OOB), fv2 = opaque(w < 6 ? v16 : OOB), fl = sopaque(L_XROW + (w < 7 ? w : 6) * 2048), wo1 = sopaque((8 + 4 * w) * 1024);
    {
      const u32 ty = (cb * 205) >> 10, cbl = cb - 5 * ty, hd = ((cbl * 32 * 205) >> 10) + (lane & 7), first = (5 * hd) >> 1, nx = (lane >> 3) & 1, cc = (rb >> 1) + nx;
      const bool val = lane < 16 && hd < 32 && first < 16 * cbl + 16 && cc < 8 && (ty > 0 || nx == 0);
      post1 = opaque(val ? (cc * 32 + hd) * 128 + (ty * 8 + ((rb & 1) + 2 - 2 * nx) * 2 + (cbl - (first >> 4))) * 4 : OOB);
    }
    VMCNT(4);
    row_stats();
    stage(1);
    f32x16 acc0 = {}, acc1 = {};
    f32x4 cs4 = {};
    float rs[2];
    u32x4 fx0[2][4], fx1[2][4], fw[2][4], xs[2], ca;
    u32x2 cg[5];
    u32 bad;
    float cs;
    auto prepA = [&]() {
      bad = (xs[0].y ^ tag) | (xs[0].w ^ tag) | (xs[1].y ^ tag) | (xs[1].w ^ tag);
#pragma unroll
      for (int k = 0; k < 5; ++k) bad |= cg[k].y ^ tag;
    };
    auto prepB = [&]() {
      cs = halves_sum(((__uint_as_float(cg[0].x) + __uint_as_float(cg[1].x)) + (__uint_as_float(cg[2].x) + __uint_as_float(cg[3].x))) + __uint_as_float(cg[4].x));
    };
    auto prepC = [&]() {
      const u32 c11 = pk(cs, cs);
      const float c2 = cs - bflo(c11);
      ca = u32x4{hh ? 0u : c11, hh ? 0u : pk(c2, c2), 0u, 0u};
      const float qs = cb < 5 ? 0.22360679774997896f * 1.4426950408889634f : 1.0f;
      rs[0] = __uint_as_float(xs[0].z) * qs; rs[1] = __uint_as_float(xs[1].z) * qs;
      xs[0] = u32x4{xs[0].x, xs[0].x, 0u, 0u}; xs[1] = u32x4{xs[1].x, xs[1].x, 0u, 0u};
    };
    u32 wov = OOB;
    auto round = [&](auto IT) __attribute__((always_inline)) {
      constexpr int it = decltype(IT)::v;
      constexpr int cu = it & 1, pv = cu ^ 1, hi = (it >> 1) & 1;
      const u32 bo = cu * 32768;
      if (it < 10) {
        if (it == 5) VMCNT(8);
        else if (it == 2 || it == 3) VMCNT(5);
        else if (it == 0 || it == 6 || it == 7) VMCNT(6);
        else if (it == 9) VMCNT(0);
        else VMCNT(4);
        if (it < 8 && it != 5) next();
        BAR();
        if (it == 0) STAMP(23);
        SB();
      }
      if (it == 8) {
        wov = *(LDS u32*)(size_t)chka;
        xs[0] = lds128(xsa); xs[1] = lds128(xsa + 512);
#pragma unroll
        for (int k = 0; k < 5; ++k) cg[k] = *(LDS u32x2*)(size_t)(csa + k * 1024);
      }
      if (it == 10) {
#pragma unroll
        for (int i = 0; i < 4; ++i) acc0 = mfma32(fw[pv][i], fx0[pv][i], acc0);
#pragma unroll
        for (int i = 0; i < 4; ++i) acc1 = mfma32(fw[pv][i], fx1[pv][i], acc1);
        return;
      }
#pragma unroll
      for (int i = 0; i < 4; ++i) {
        if (it > 0) acc0 = mfma32(fw[pv][i], fx0[pv][i], acc0);
        SB();
        fw[cu][i] = ldstr(aw[hi][0] + bo + i * 4096, aw[hi][1] + bo + i * 4096);
        SB();
        if (it > 0) acc1 = mfma32(fw[pv][i], fx1[pv][i], acc1);
        SB();
        fx0[cu][i] = lds128(ax[hi][i] + bo); fx1[cu][i] = lds128(ax[hi][i] + bo + 4096);
        SB();
        if (it == 1) cs4 = mfma16(sel, fw[pv][i], cs4);
        if (it == 5 && i == 0) { glds_dev(rws, fv, fso, fl); glds_dev(rws, fv2, fso + 1024, fl + 1024); }
        if (it == 4) { if (i == 2) next(); piece(2 + (i >> 1), (2 * i) & 3); piece(2 + (i >> 1), (2 * i + 1) & 3); }
        else if (it < 8 && it != 5) piece((it + 2) & 3, i);
        if (it == 9 && i == 0) glds4(rws, wov, OFF_WO + cb3 * 102400 + wo1, L_WO + wo1);
        if (it == 8) {
          if (i == 1) { prepA(); asm volatile("" : "+v"(bad)); }
          if (i == 2) { prepB(); woready = __all(wov == tag); wov = woready ? v16 : OOB; asm volatile("" : "+v"(cs), "+v"(wov)); }
          if (i == 3) { prepC(); asm volatile("" : "+v"(ca), "+v"(xs[0]), "+v"(xs[1]), "+v"(rs[0]), "+v"(rs[1])); }
        }
        SB();
      }
      if (it == 1) {
        const u32x2 gq = {__float_as_uint(cs4[0] + 0.f), tag};
        asm volatile("buffer_store_dwordx2 %0, %1, %2, %3 offen sc1" ::"v"(gq), "v"(csv), "s"(rws), "s"(cso) : "memory");
        SB();
      }
      if (it == 8) {
        if (__builtin_expect(__any(bad != 0), 0)) {
          u32 n = 0;
#pragma unroll 1
          do {
            __builtin_amdgcn_s_sleep(8);
            xs[0] = ld_dev(rws, l31 * 16, OFF_XS + (128 * rb + 64 * wm) * 16);
            xs[1] = ld_dev(rws, l31 * 16, OFF_XS + (128 * rb + 64 * wm + 32) * 16);
#pragma unroll
            for (int k = 0; k < 5; ++k) cg[k] = __builtin_amdgcn_raw_buffer_load_b64(rws, hh * 5120 + l31 * 8, OFF_CSQ + (cb * 10 + k) * 1024 + wn * 256, 16);
            __builtin_amdgcn_s_waitcnt(0x0F70);
            asm volatile("" ::: "memory");
            prepA();
          } while (__any(bad != 0) && ++n < 65536);
          if (n >= 65536) give_up(rws);
          prepB();
          prepC();
        }
        acc0 = mfma32(ca, xs[0], acc0);
        acc1 = mfma32(ca, xs[1], acc1);
        SB();
      }
    };
    round(IC<0>{}); round(IC<1>{}); round(IC<2>{}); round(IC<3>{}); round(IC<4>{}); round(IC<5>{});
    round(IC<6>{}); round(IC<7>{}); round(IC<8>{}); round(IC<9>{}); round(IC<10>{});
    STAMP(1);
#pragma unroll
    for (int mt = 0; mt < 2; ++mt) {
      u32 d[8];
#pragma unroll
      for (int u = 0; u < 8; ++u) d[u] = pk((mt ? acc1[2 * u] : acc0[2 * u]) * rs[mt], (mt ? acc1[2 * u + 1] : acc0[2 * u + 1]) * rs[mt]);
      st_dev(widen32(d[0], d[1], d[2], d[3]), rws, v16, so1 + mt * GS_QKV);
      st_dev(widen32(d[4], d[5], d[6], d[7]), rws, v16, so1 + mt * GS_QKV + 1024);
    }
    STAMP(2);
  } else {
    const u32 sidx = pr * 4 + (j - 28);
    const rsrc_t rr = mkrsrc(pwo, 640 * 1280);
    side_loads();
    const u32 vo = (lane >> 3) * 1280 + (lane & 7) * 16;
#pragma unroll
    for (int k = 0; k < 2; ++k) {
      const u32 u = sidx * 8 + w + k * 128, kb = (u * 6554) >> 16, nb = u - kb * 10;
#pragma unroll
      for (int i = 0; i < 4; ++i) glds_nt(rr, u < 200 ? vo : OOB, (32 * kb + 8 * i) * 1280 + nb * 128, w * 8192 + k * 4096 + i * 1024);
    }
    if (tid < 32) *(LDS u32*)(size_t)(L_CTRL + tid * 4) = 0;
    VMCNT(8);
    row_stats();
    VMCNT(2);
    const u32 ta = w * 8192 + (8 * g4 + q4) * 128 + 8 * p4;
#pragma unroll
    for (int k = 0; k < 2; ++k) {
      const u32 u = sidx * 8 + w + k * 128, kb = (u * 6554) >> 16, nb = u - kb * 10;
#pragma unroll
      for (int f = 0; f < 4; ++f) {
        const u32 ctile = 4 * nb + f, cbk = (ctile * 205) >> 10, ct = ctile - cbk * 5;
        st_dev(ldstr(ta + k * 4096 + 32 * f, ta + k * 4096 + 32 * f + 512), rws, u < 200 ? v16 : OOB, OFF_WO + ((cbk * 20 + kb) * 5 + ct) * 1024);
      }
    }
    VMCNT(0);
    BAR();
    if (w < 4) post(rws, lane * 128, OFF_MBW + w * 8192 + sidx * 4, tag);
    await(rws, v4, OFF_MBW + b * 128, tag, 0xffffull);
    glds4(rws, v16, OFF_WO + (cb3 * 100 + 8 + 4 * w) * 1024, L_WO + (8 + 4 * w) * 1024);
    woready = true;
    __builtin_amdgcn_s_sleep(SPARE_SLEEP);
  }
  DRAIN();
  STAMP(3);
  BAR();
  if (w == 0) post(rws, post1, OFF_MB1, tag);
  if (w == 1) set_flag(rws, OFF_SEEN + b * 4, tag);
  f32x16 c0, c8;
  {
    const int e = (int)l31 - 4 * (int)hh;
#pragma unroll
    for (int r = 0; r < 16; ++r) { const bool cnd = 8 * (r >> 2) + (r & 3) > e; c0[r] = cnd ? 0.f : -1e30f; c8[r] = cnd ? -1e30f : 0.f; }
    asm volatile("" : "+v"(c0), "+v"(c8));
  }
  const u32 ka = opaque(hh * KVS + (32 * w + l31) * 16), kb = opaque((2 + 4 * hh) * KVS + (32 * w + l31) * 16);
  const u32 va = opaque((blk ? 5 + (p4 & 1) : 3 + (p4 >> 1)) * KVS + 8 * (blk ? odd & ~p4 : p4 & 1) + (32 * w + 4 * hh + q4) * 16);
  const u32 vkv = opaque(hh * GS_QKV + l31 * 16), vlo = opaque(hh ? OOB : v16);
  const u32 sq = sopaque(OFF_QKV + (8 * c + w) * GS_QKV), skv = sopaque(OFF_QKV + ((c == 0 && w < 4) ? 2 * w : 8 * c - 8 + 2 * w) * GS_QKV);
  const u32 pA = sopaque(plA * 512), pC = sopaque(plC * 512);
  u64 need1;
  {
    const u32 first = pb + 2 * odd;
    need1 = __ballot(lane < 24 && (c > 0 || (lane & 4)) && (lane >= 8 || (lane & 4)) && (!(lane & 1) || ((first + 2) >> 4) != (first >> 4)));
  }
  const u32 ru = (pr * 4 + (j >> 3)) * 4 + w4, rkb = (ru * 43) >> 7, rct = ru - 3 * rkb, rF = (pr * 8 + (j >> 2)) * 8 + 2 * w4;
  u32x2 xr[2][2];
  *(LDS u32x4*)(size_t)(6 * KVS + tid * 16) = u32x4{0x3f803f80u, 0x3f80u, (c == 0 && tid < 256) ? 0xf149u : 0u, 0u};
  if (w == 0) await(rws, v4, mb1, tag, need1);
  if (__builtin_expect(!woready, 0)) {
    await(rws, v4, OFF_MBW + b * 128, tag, 0xffffull);
    glds4(rws, v16, OFF_WO + (cb3 * 100 + 8 + 4 * w) * 1024, L_WO + (8 + 4 * w) * 1024);
  }
  BAR();
  STAMP(4);
  u32x4 q1 = ld_raw(rws, v16, sq + pA), q2 = ld_raw(rws, vlo, sq + pC);
  u32x4 k1 = ld_raw(rws, v16, sq + pA + 40960), k2 = ld_raw(rws, vlo, sq + pC + 40960);
  glds_dev(rws, vkv, skv + pA + 40960, w * 1024);
  glds_dev(rws, vkv, skv + pA + 41472, w * 1024 + KVS);
  glds_dev(rws, vkv, skv + pC + 40960, w * 1024 + 2 * KVS);
  glds_dev(rws, vkv, skv + pA + 81920, w * 1024 + 3 * KVS);
  glds_dev(rws, vkv, skv + pA + 82432, w * 1024 + 4 * KVS);
  glds_dev(rws, vkv, skv + pC + 81920, w * 1024 + 5 * KVS);
  if (half) {
    VMCNT(0);
    BAR();
    STAMP(5);
    const rsrc_t r1 = mkrsrc(pw1, 640 * 3072), r2 = mkrsrc(pw2, 1536 * 1280);
    const u32 v1 = ru < 60 ? (lane >> 2) * 3072 + p4 * 16 : OOB, v2 = rF < 240 ? (lane >> 1) * 1280 + (lane & 1) * 16 : OOB;
#pragma unroll
    for (int f = 0; f < 2; ++f) {
      const u32 k2 = ((rF + f) * 205) >> 10;
      glds_nt(r1, v1, (2 * rkb + f) * 49152 + cb4 * 192 + rct * 64, L_RELAY + w4 * 4096 + f * 1024);
      glds_nt(r2, v2, k2 * 40960 + cb3 * 160 + (rF + f - 5 * k2) * 32, L_RELAY + w4 * 4096 + 2048 + f * 1024);
    }
  }
  asm volatile("s_waitcnt vmcnt(6)" : "+v"(q1), "+v"(q2), "+v"(k1), "+v"(k2)::"memory");
  if (odd) { q2.x = 0u; q2.y = 0u; } else { q2.z = 0u; q2.w = 0u; }
  float shift;
  {
    float d = dot2(k1.x, q1.x, 0.f), d2 = dot2(k2.x, q2.x, 0.f);
    d = dot2(k1.y, q1.y, d); d2 = dot2(k2.y, q2.y, d2);
    d = dot2(k1.z, q1.z, d); d2 = dot2(k2.z, q2.z, d2);
    d = dot2(k1.w, q1.w, d); d2 = dot2(k2.w, q2.w, d2);
    shift = halves_sum(d + d2);
  }
  q2.z |= hh ? 0x3f80u : 0u;
  k2.x |= hh ? 0x3f803f80u : 0u;
  k2.y |= hh ? 0x3f80u : 0u;
  u32x4 w2f[6][5];
  const u32 w2o = sopaque(OFF_W2 + (cb3 * 48 + 6 * w) * 5120);
  u32x4 ow;
  u32x2 oc;
#pragma unroll 1
  for (int attempt = 0;; ++attempt) {
    u32x4 q3 = q2;
    {
      const float h1 = bflo(pk(-shift, 0.f)), r1 = -shift - h1, h2 = bflo(pk(r1, 0.f));
      q3.x = hh ? pk(h1, h2) : q2.x;
      q3.y = hh ? pk(r1 - h2, 0.f) : q2.y;
    }
    const f32x16 z = {};
    f32x16 sc = mfma32(k1, q1, c8), sn, o = z;
    sc = mfma32(k2, q3, sc);
    if (attempt == 0 && !half) {
      SB();
      VMCNT(0);
      BAR();
      STAMP(5);
      __builtin_amdgcn_s_setprio(1);
    }
    u32x4 ka1 = lds128(ka), ka2 = lds128(kb), vf[2];
#pragma unroll
    for (int i = 0; i < 9; ++i) {
      const int t = i == 0 ? 8 : i - 1;
      SB();
      vf[0] = ldstr(va + t * 512, va + t * 512 + 128);
      vf[1] = ldstr(va + t * 512 + 256, va + t * 512 + 384);
      if (i > 0 && i < 8) {
        sn = mfma32(ka1, q1, z);
        sn = mfma32(ka2, q3, sn);
        if (i < 7) { ka1 = lds128(ka + (i + 1) * 512); ka2 = lds128(kb + (i + 1) * 512); }
      }
      if (half && attempt == 0) {
        if (i >= 1 && i <= 3) {
          const u32 f = i < 3 ? 24 + 16 * i + 4 * w4 : w4 < 3 ? 72 + 4 * w4 : 0;
          glds4(rws, v16, OFF_WO + (cb3 * 100 + f) * 1024, L_WO + f * 1024);
          if (i == 3 && w4 == 0) glds4(rws, v16, OFF_WO + (cb3 * 100 + 4) * 1024, L_WO + 4096);
        }
        if (i == 4) {
          VMCNT(12);
          const u32 a1 = L_RELAY + w4 * 4096 + (8 * hh + q4) * 64 + blk * 32 + 8 * p4, a2 = L_RELAY + w4 * 4096 + 2048 + (8 * g4 + q4) * 32 + 8 * p4;
          const u32x4 r0 = ldstr(a1, a1 + 256), r1 = ldstr(a1 + 1024, a1 + 1280), r2 = ldstr(a2, a2 + 128), r3 = ldstr(a2 + 1024, a2 + 1152);
          f32x4 t = {};
          t = mfma16(sel, r0, t);
          t = mfma16(sel, r1, t);
          const u32 v1 = ru < 60 ? v16 : OOB, v2 = rF < 240 ? v16 : OOB;
          st_dev(r0, rws, v1, OFF_W1 + ((cb4 * 40 + 2 * rkb) * 3 + rct) * 1024);
          st_dev(r1, rws, v1, OFF_W1 + ((cb4 * 40 + 2 * rkb + 1) * 3 + rct) * 1024);
          st_dev(r2, rws, v2, OFF_W2 + (cb3 * 240 + rF) * 1024);
          st_dev(r3, rws, v2, OFF_W2 + (cb3 * 240 + rF + 1) * 1024);
          const float t0 = t[0] + 0.f;
          asm volatile("buffer_store_dword %0, %1, %2, %3 offen sc1" ::"v"(t0), "v"((ru >= 60 || hh) ? OOB : v4), "s"(rws),
                       "s"(OFF_CSP + rkb * 6144 + (cb4 * 3 + rct) * 128) : "memory");
          glds4(rws, v16, OFF_WO + (cb3 * 100 + 84 + 4 * w4) * 1024, L_RELAY + w4 * 4096);
        }
        if (i == 6) {
          VMCNT(0);
          if (lds_ticket(C_WO) == 3) post(rws, lane < 32 ? ((lane & 7) * 32 + (lane >> 3) * 2) * 128 : OOB, OFF_MB0 + ((j & 3) * 8 + xo) * 128 + (pr * 8 + (j >> 2)) * 4, tag);
        }
      }
      if (i == 6) {
#pragma unroll
        for (int pp = 0; pp < 2; ++pp)
#pragma unroll
          for (int m2 = 0; m2 < 2; ++m2)
            xr[pp][m2] = __builtin_amdgcn_raw_buffer_load_b64(rx, (pp && w4) ? OOB : (16 * m2 + l15) * 1280 + g4 * 8, gr * 40960 + 160 * cb3 + 32 * (pp ? 4 : w4), 0);
      }
      SB();
#pragma unroll
      for (int u = 0; u < 2; ++u) {
        float p[8];
#pragma unroll
        for (int r = 0; r < 8; ++r) p[r] = __builtin_amdgcn_exp2f(sc[8 * u + r]);
        if (i == 0 && u == 0) {
          SB();
          sn = mfma32(ka1, q1, c0);
          sn = mfma32(ka2, q3, sn);
          ka1 = lds128(ka + 512); ka2 = lds128(kb + 512);
          SB();
        }
        o = mfma32(vf[u], u32x4{pk(p[0], p[1]), pk(p[2], p[3]), pk(p[4], p[5]), pk(p[6], p[7])}, o);
      }
      sc = sn;
    }
    SB();
    float lsum;
    { auto r = __builtin_amdgcn_permlane32_swap(__float_as_uint(o[8]), __float_as_uint(o[8]), false, false); lsum = __uint_as_float(r[1]); }
    const float inv = __builtin_amdgcn_rcpf(lsum);
    const f32x2 chk = ((f32x2{o[0], o[1]} + f32x2{o[2], o[3]}) + (f32x2{o[4], o[5]} + f32x2{o[6], o[7]})) + (f32x2{o[8], o[9]} + f32x2{o[10], o[11]});
    const bool ok = lsum <= 1e15f && lsum >= 0.25f && fabsf(chk.x + chk.y) <= 3e38f;
    ow = widen32(pk(o[0] * inv, o[1] * inv), pk(o[2] * inv, o[3] * inv), pk(o[4] * inv, o[5] * inv), pk(o[6] * inv, o[7] * inv));
    oc = u32x2{pk(o[8] * inv, o[9] * inv), pk(o[10] * inv, o[11] * inv)};
    asm volatile("" : "+v"(ow), "+v"(oc));
    if (__builtin_expect(__all(ok) || attempt, 1)) break;
    float m = -3e38f;
#pragma unroll 1
    for (int t = 0; t < 9; ++t) {
      f32x16 sv = mfma32(lds128(ka + t * 512), q1, z);
      sv = mfma32(lds128(kb + t * 512), q2, sv);
#pragma unroll
      for (int r = 0; r < 16; ++r) m = fmaxf(m, sv[r] + (t == 0 ? c0[r] : t == 8 ? c8[r] : 0.f));
    }
    auto r = __builtin_amdgcn_permlane32_swap(__float_as_uint(m), __float_as_uint(m), false, false);
    shift = fmaxf(__uint_as_float(r[0]), __uint_as_float(r[1]));
  }
  st_dev(ow, rws, v16, OFF_O + (8 * c + w) * GS_O + pA);
  __builtin_amdgcn_raw_buffer_store_b64(oc, rws, vlo, OFF_O + (8 * c + w) * GS_O + pC + 8 * odd, 16);
  STAMP(6);

  const u32 vq = opaque(g4 * 512 + l15 * 16);
  const u32 s0l = opaque(L_S0 + v16);
  f32x4 acc[2][5] = {};
  {
    const u32 pv2 = lane < 8 ? lane * 512 : OOB, ps2 = OFF_MB2 + ((8 * c + w) * 32 + (head >> 3)) * 128 + (head & 7) * 4;
    if (!half) { DRAIN(); post(rws, pv2, ps2, tag); STAMP(7); }
    u32x4 wf[5][5];
    lds_wait(C_WO, 4);
#pragma unroll
    for (int cc = 0; cc < 5; ++cc) {
      const u32 a = opaque(L_WO + w4 * 25600 + (cc < 4 ? ((w4 + cc) & 3) : 4) * 1024 + v16);
#pragma unroll
      for (int si = 0; si < 5; ++si) wf[si][cc] = lds128(a + si * 5120);
    }
    SB();
    lds_inc(C_FREE);
    if (half) { DRAIN(); post(rws, pv2, ps2, tag); STAMP(7); }
    if (b == 0 && w == 3) {
      u32 n = 0;
#pragma unroll 1
      for (;;) {
        const u32x4 f = ld_dev(rws, v16, OFF_SEEN);
        asm volatile("" ::: "memory");
        if (__all(f.x == tag && f.y == tag && f.z == tag && f.w == tag) || ++n > 16384) break;
        __builtin_amdgcn_s_sleep(32);
      }
      set_flag(rws, OFF_EPOCH, tag);
    }
#pragma unroll
    for (int m2 = 0; m2 < 2; ++m2) {
      acc[m2][0] = f32x4{bflo(xr[0][m2].x), bfhi(xr[0][m2].x), bflo(xr[0][m2].y), bfhi(xr[0][m2].y)};
      acc[m2][4] = f32x4{bflo(xr[1][m2].x), bfhi(xr[1][m2].x), bflo(xr[1][m2].y), bfhi(xr[1][m2].y)};
      asm volatile("" : "+v"(acc[m2][0]), "+v"(acc[m2][4]));
    }
    const u32 so = sopaque(OFF_O + gr * GS_O + w4 * 10240);
    if (half) await(rws, v4, OFF_MB0 + b * 128, tag, 0xffffffffull);
    await(rws, v4, mb2, tag, 0xffull);
    STAMP(8);
    u32x4 af[5][2];
#pragma unroll
    for (int n = 0; n < 10; ++n) af[n >> 1][n & 1] = ld_at(rws, vq, so, (n >> 1) * 2048 + (n & 1) * 256);
    SB();
    lds_wait(half ? C_SCR : C_FREE, half ? 4 : 8);
#pragma unroll
    for (int si = 0; si < 5; ++si)
#pragma unroll
      for (int m2 = 0; m2 < 2; ++m2)
#pragma unroll
        for (int cc = 0; cc < 5; ++cc) acc[m2][cc] = mfma16(wf[si][cc], af[si][m2], acc[m2][cc]);
  }
  STAMP(9);
  if (half) {
    SB();
#pragma unroll
    for (int i = 0; i < 2; ++i) {
      glds3(rws, v16, OFF_W1 + cb4 * 122880 + (i * 60 + 6 * w4) * 1024, (i * 60 + 6 * w4) * 1024);
      glds3(rws, v16, OFF_W1 + cb4 * 122880 + (i * 60 + 6 * w4 + 3) * 1024, (i * 60 + 6 * w4 + 3) * 1024);
    }
  }
#pragma unroll
  for (int cc = 1; cc < 5; ++cc) {
    const u32 a = s0l + (cc < 4 ? (((w4 + cc) & 3) * 3 + cc - 1) * 2048 : 24576 + w4 * 2048);
    lds_st4(a, acc[0][cc]);
    lds_st4(a + 1024, acc[1][cc]);
  }
  lds_inc(C_X3 + half * 4);
  lds_wait(C_X3 + half * 4, 4);
  u32 hp[2][2][2];
#pragma unroll
  for (int pp = 0; pp < 2; ++pp) {
    if (pp == 0 || w4 == 0) {
      const u32 a = pp ? s0l + 24576 + 2048 : s0l + w4 * 6144;
#pragma unroll
      for (int m2 = 0; m2 < 2; ++m2) {
        const f32x4 v = (acc[m2][pp ? 4 : 0] + ldsf4(a + m2 * 1024)) + (ldsf4(a + 2048 + m2 * 1024) + ldsf4(a + 4096 + m2 * 1024));
        hp[pp][m2][0] = pk(v.x, v.y);
        hp[pp][m2][1] = pk(v.z, v.w);
      }
      st_dev(widen16(hp[pp][0][0], hp[pp][0][1], hp[pp][1][0], hp[pp][1][1]), rws, v16, OFF_H + gr * GS_O + (5 * cb3 + (pp ? 4 : w4)) * 1024);
    } else {
      hp[pp][0][0] = hp[pp][0][1] = hp[pp][1][0] = hp[pp][1][1] = 0u;
    }
  }
  lds_inc(C_SCR);
  STAMP(10);
  {
    float sv[2], qv[2];
#pragma unroll
    for (int m2 = 0; m2 < 2; ++m2) {
      sv[m2] = 0.f; qv[m2] = 0.f;
#pragma unroll
      for (int pp = 0; pp < 2; ++pp)
#pragma unroll
        for (int k = 0; k < 2; ++k) { sv[m2] = dot2(hp[pp][m2][k], 0x3f803f80u, sv[m2]); qv[m2] = dot2(hp[pp][m2][k], hp[pp][m2][k], qv[m2]); }
    }
    auto r1 = __builtin_amdgcn_permlane16_swap(__float_as_uint(sv[0]), __float_as_uint(sv[1]), false, false);
    auto r2 = __builtin_amdgcn_permlane16_swap(__float_as_uint(qv[0]), __float_as_uint(qv[1]), false, false);
    auto r3_ = __builtin_amdgcn_permlane32_swap(__float_as_uint(__uint_as_float(r1[0]) + __uint_as_float(r1[1])),
                                                __float_as_uint(__uint_as_float(r2[0]) + __uint_as_float(r2[1])), false, false);
    *(LDS float*)(size_t)(L_HS + w * 256 + lane * 4) = __uint_as_float(r3_[0]) + __uint_as_float(r3_[1]);
  }
  DRAIN();
  if (half) lds_inc(C_W1);
  if (lds_ticket(C_HS + half * 4) == 3) {
    post(rws, lane < 16 ? lane * 256 : OOB, OFF_MB3 + (gr * 32 + (cb3 >> 2)) * 128 + (cb3 & 3) * 4, tag);
    const u32 a = opaque(L_HS + half * 1024 + lane * 4);
    const float t = (*(LDS float*)(size_t)a + *(LDS float*)(size_t)(a + 256)) + (*(LDS float*)(size_t)(a + 512) + *(LDS float*)(size_t)(a + 768));
    __builtin_amdgcn_raw_buffer_store_b64(u32x2{__float_as_uint(t), tag}, rws, lane * 8, OFF_HS + (gr * 8 + cb3) * 512, 16);
  }
  STAMP(11);
  if (!half) {
    await(rws, v4, OFF_MB0 + b * 128, tag, 0xffffffffull);
#pragma unroll
    for (int i = 0; i < 6; ++i) glds3(rws, v16, OFF_W1 + cb4 * 122880 + ((i / 3) * 60 + 24 + 9 * w4 + 3 * (i % 3)) * 1024, ((i / 3) * 60 + 24 + 9 * w4 + 3 * (i % 3)) * 1024);
    if (w < 3) {
      const u32 col = cb4 * 96 + w * 32;
      float t[10];
#pragma unroll
      for (int k = 0; k < 10; ++k) t[k] = __uint_as_float(__builtin_amdgcn_raw_buffer_load_b32(rws, hh * 61440 + l31 * 4, OFF_CSP + col * 4 + k * 6144, 16));
      const u32 bv = __builtin_amdgcn_raw_buffer_load_b16(mkrsrc(pb1, 3072), l31 * 2, col * 2, 0);
      const float cs = halves_sum(((t[0] + t[1]) + (t[2] + t[3])) + ((t[4] + t[5]) + (t[6] + t[7])) + (t[8] + t[9]));
      const u32 c11 = pk(cs, cs);
      const float c2 = cs - bflo(c11);
      *(LDS u32x4*)(size_t)(L_AUG + w * 512 + l31 * 16) = u32x4{c11, pk(c2, c2), bv * 0x10001u, 0u};
    }
  }

  {
    const u32 so = sopaque(OFF_H + g4r * GS_O + kh * 20480);
    if (!half) { VMCNT(0); lds_inc(C_W1); }
    await(rws, v4, mb3, tag, 0xfull);
    STAMP(12);
    u32x4 hf[4][5];
#pragma unroll
    for (int n = 0; n < 20; ++n) hf[n / 5][n % 5] = ld_at(rws, v16, so, n * 1024);
#pragma unroll
    for (int n = 0; n < 12; ++n) w2f[n / 5][n % 5] = ld_at(rws, v16, w2o, n * 1024);
    u32x2 hs[8];
    const u32 hso = OFF_HS + g4r * 4096, v8 = opaque(lane * 8);
    auto load_hs = [&]() {
#pragma unroll
      for (int k = 0; k < 8; ++k) hs[k] = __builtin_amdgcn_raw_buffer_load_b64(rws, v8 + k * 512, hso, 16);
    };
    lds_wait(C_W1, 8);
    STAMP(15);
    f32x16 a4[3] = {};
    const u32 wa = kh * 61440 + (lane ^ (kh << 4)) * 16, wx = opaque(L_AUG + (l31 ^ (kh << 4)) * 16);
    u32x4 ring[6];
#pragma unroll
    for (int n = 0; n < 6; ++n) ring[n] = lds128(wa + n * 1024);
    float rs;
    u32 tp, bad;
    auto stats = [&]() {
      bad = 0;
#pragma unroll
      for (int k = 0; k < 8; ++k) bad |= hs[k].y ^ tag;
      const float tot = ((__uint_as_float(hs[0].x) + __uint_as_float(hs[1].x)) + (__uint_as_float(hs[2].x) + __uint_as_float(hs[3].x))) +
                        ((__uint_as_float(hs[4].x) + __uint_as_float(hs[5].x)) + (__uint_as_float(hs[6].x) + __uint_as_float(hs[7].x)));
      const auto rt = __builtin_amdgcn_permlane32_swap(__float_as_uint(tot), __float_as_uint(tot), false, false);
      const float mu = __uint_as_float(rt[0]) * (1.0f / 640.0f);
      const float var = fmaxf(__uint_as_float(rt[1]) * (1.0f / 640.0f) - mu * mu, 0.f);
      const float v1 = var + 1e-5f;
      const float d2 = v1 * (var * __builtin_amdgcn_rcpf(v1) + 1e-5f);
      rs = __builtin_amdgcn_rsqf(d2);
      const float tv = kh ? d2 * rs : -mu;
      const float t1 = bflo(pk(tv, 0.f));
      tp = hh ? 0u : pk(t1, tv - t1);
    };
#pragma unroll
    for (int cc = 0; cc < 4; ++cc) {
      if (cc == 3) stats();
#pragma unroll
      for (int n = 15 * cc; n < 15 * cc + 15; ++n) {
        if (cc < 3) SB();
        a4[n % 3] = mfma32(ring[n % 6], hf[cc][(n % 15) / 3], a4[n % 3]);
        if (n + 6 < 60) ring[n % 6] = lds128(wa + (n + 6) * 1024);
        else if (n + 6 < 63) ring[n % 6] = lds128(wx + (n + 6 - 60) * 512);
      }
      if (cc == 3) {
#pragma unroll
        for (int n = 0; n < 15; ++n) {
          __builtin_amdgcn_sched_group_barrier(0x8, 1, 0);
          __builtin_amdgcn_sched_group_barrier(0x100, 1, 0);
          __builtin_amdgcn_sched_group_barrier(0x402, 4, 0);
        }
      }
      SB();
      if (cc == 0) { load_hs(); SB(); }
      if (cc == 2) { lds_wait(C_SCR, 8 + 4 * half); SB(); }
    }
    if (__builtin_expect(__any(bad != 0), 0)) {
      u32 it = 0;
#pragma unroll 1
      do {
        load_hs();
        asm volatile("" ::: "memory");
        stats();
      } while (__any(bad != 0) && ++it < 4096);
      if (it >= 4096) give_up(rws);
    }
    {
      const u32x4 bx = {kh ? 0u : tp, kh ? 0u : tp, kh ? tp : 0u, 0u};
#pragma unroll
      for (int ct = 0; ct < 3; ++ct) a4[ct] = mfma32(ring[ct], bx, a4[ct]);
    }
    STAMP(13);
    lds_inc(C_FREE2);
    {
      const u32 a = s0l + w4 * 6144;
#pragma unroll
      for (int ct = 0; ct < 3; ++ct) {
        lds_st4(a + ct * 2048, f32x4{a4[ct][8], a4[ct][9], a4[ct][10], a4[ct][11]});
        lds_st4(a + ct * 2048 + 1024, f32x4{a4[ct][12], a4[ct][13], a4[ct][14], a4[ct][15]});
      }
    }
    lds_inc(C_X4 + (w >> 1) * 4);
#pragma unroll
    for (int n = 12; n < 30; ++n) w2f[n / 5][n % 5] = ld_at(rws, v16, w2o, n * 1024);
    lds_wait(C_X4 + (w >> 1) * 4, 2);
    STAMP(18);
    const u32 oa = s0l + (w4 ^ 1) * 6144;
    f32x4 ot[6];
#pragma unroll
    for (int k = 0; k < 6; ++k) ot[k] = ldsf4(oa + k * 1024);
    SB();
    lds_inc(C_SCR);
#pragma unroll
    for (int ct = 0; ct < 3; ++ct) {
      u32 d[4];
#pragma unroll
      for (int g = 0; g < 4; ++g) {
        const f32x2 u = (f32x2{a4[ct][2 * g], a4[ct][2 * g + 1]} + f32x2{ot[2 * ct + (g >> 1)][(2 * g) & 3], ot[2 * ct + (g >> 1)][(2 * g + 1) & 3]}) * rs;
        const f32x2 z = u * (u * u * -0.1029432397f + -2.302208198f);
        const f32x2 dn = f32x2{__builtin_amdgcn_exp2f(z.x), __builtin_amdgcn_exp2f(z.y)} + 1.0f;
        const f32x2 y = u * f32x2{__builtin_amdgcn_rcpf(dn.x), __builtin_amdgcn_rcpf(dn.y)};
        d[g] = pk(y.x, y.y);
      }
      st_dev(widen32(d[0], d[1], d[2], d[3]), rws, v16, OFF_H1 + g4r * GS_H1 + (6 * cb4 + 2 * ct + kh) * 1024);
    }
    STAMP(14);
    if (half) await(rws, v4, mb4, tag, 0xfull);
    DRAIN();
    post(rws, lane < 8 ? lane * 1024 : OOB, OFF_MB4 + (g4r * 64 + (cb4 >> 1)) * 128 + ((cb4 & 1) * 2 + kh) * 4, tag);
  }
  u32x2 b2r[2];
#pragma unroll
  for (int pp = 0; pp < 2; ++pp) b2r[pp] = __builtin_amdgcn_raw_buffer_load_b64(mkrsrc(pb2, 1280), 8 * g4, 160 * cb3 + 32 * (pp ? 4 : w4), 0);
  __builtin_amdgcn_s_setprio(0);

  const rsrc_t ro = mkrsrc(pout, 2048 * 1280);
  u32x4 af[12];
  const u32 so5 = sopaque(OFF_H1 + g3 * GS_H1 + w * 12288);
  if (!half) await(rws, v4, mb4, tag, 0xfull);
  STAMP(16);
#pragma unroll
  for (int n = 0; n < 12; ++n) af[n] = ld_at(rws, vq, so5, (n >> 1) * 2048 + (n & 1) * 256);
  SB();
#pragma unroll
  for (int X = 0; X < 2; ++X) {
    f32x4 a5[2][5] = {};
#pragma unroll
    for (int n = 0; n < 12; ++n)
#pragma unroll
      for (int ct = 0; ct < 5; ++ct) a5[n & 1][ct] = mfma16(w2f[n >> 1][ct], af[n], a5[n & 1][ct]);
    if (X == 0) lds_wait(C_FREE2, 8);
    if (X == 0) STAMP(17); else STAMP(20);
    {
      const u32 a0 = opaque(X * 81920 + w * 1024 + v16), a1 = opaque(X * 81920 + 49152 + w * 1024 + v16);
#pragma unroll
      for (int ct = 0; ct < 5; ++ct)
#pragma unroll
        for (int m2 = 0; m2 < 2; ++m2) lds_st4(ct < 3 ? a0 + (ct * 2 + m2) * 8192 : a1 + (ct * 2 + m2 - 6) * 8192, a5[m2][ct]);
    }
    if (X == 0) {
      await(rws, v4, mb4 + 32768, tag, 0xfull);
#pragma unroll
      for (int n = 0; n < 12; ++n) af[n] = ld_at(rws, vq, so5 + 4 * GS_H1, (n >> 1) * 2048 + (n & 1) * 256);
      SB();
    }
    BAR();
    if (X == 0) STAMP(19); else STAMP(21);
    if (half == X) {
#pragma unroll
      for (int pp = 0; pp < 2; ++pp) {
        if (pp == 0 || w4 == 0) {
          const u32 ct = pp ? 4 : w4;
          const u32 a = opaque(X * 81920 + ct * 16384 + v16);
          u32 d[2][2];
#pragma unroll
          for (int m2 = 0; m2 < 2; ++m2) {
            f32x4 t[8];
#pragma unroll
            for (int k = 0; k < 8; ++k) t[k] = ldsf4(a + m2 * 8192 + k * 1024);
            f32x4 v = {bflo(hp[pp][m2][0]) + bflo(b2r[pp].x), bfhi(hp[pp][m2][0]) + bfhi(b2r[pp].x), bflo(hp[pp][m2][1]) + bflo(b2r[pp].y),
                       bfhi(hp[pp][m2][1]) + bfhi(b2r[pp].y)};
            v += ((t[0] + t[1]) + (t[2] + t[3])) + ((t[4] + t[5]) + (t[6] + t[7]));
            d[m2][0] = pk(v.x, v.y);
            d[m2][1] = pk(v.z, v.w);
          }
          st_plain(widen16(d[0][0], d[0][1], d[1][0], d[1][1]), ro, l31 * 1280 + hh * 16, gr * 40960 + (80 * cb3 + 16 * ct) * 2);
        }
      }
    }
  }
}

extern "C" void kernel_launch(void* const* d_in, const int* in_sizes, int n_in, void* d_out, int out_size, void* d_ws, size_t ws_size,
                              hipStream_t stream) {
  fused_block<<<256, 512, 0, stream>>>(d_in[0], d_in[1], d_in[2], d_in[4], d_in[5], d_in[6], d_in[7], d_out, d_ws);
}
```

```cpp
#include <hip/hip_runtime.h>

typedef unsigned int u32;
typedef unsigned long long u64;
typedef __bf16 bf16x2 __attribute__((ext_vector_type(2)));
typedef __bf16 bf16x8 __attribute__((ext_vector_type(8)));
typedef short s16x4 __attribute__((ext_vector_type(4)));
typedef float f32x2 __attribute__((ext_vector_type(2)));
typedef float f32x4 __attribute__((ext_vector_type(4)));
typedef float f32x16 __attribute__((ext_vector_type(16)));
typedef u32 u32x2 __attribute__((ext_vector_type(2)));
typedef u32 u32x4 __attribute__((ext_vector_type(4)));
typedef __amdgpu_buffer_rsrc_t rsrc_t;
#define LDS __attribute__((address_space(3)))
#define DI __device__ __forceinline__

constexpr u32 GS_QKV = 240 * 512, GS_O = 80 * 512, GS_H1 = 192 * 512;
constexpr u32 OFF_EPOCH = 0, OFF_ERR = 128, OFF_SEEN = 1024, OFF_XS = 16384, OFF_CSQ = 65536;
constexpr u32 OFF_MB1 = OFF_CSQ + 160 * 1024, OFF_MB0 = OFF_MB1 + 256 * 128, OFF_MBW = OFF_MB0 + 256 * 128;
constexpr u32 OFF_MB = OFF_MBW + 272 * 128;
constexpr u32 OFF_HS = OFF_MB + 2048 * 128;
constexpr u32 OFF_QKV = OFF_HS + 512 * 512, OFF_O = OFF_QKV + 64 * GS_QKV, OFF_H = OFF_O + 64 * GS_O, OFF_H1 = OFF_H + 64 * GS_O;
constexpr u32 OFF_WO = OFF_H1 + 64 * GS_H1, OFF_W2 = OFF_WO + 800 * 1024;
constexpr u32 WS_END = OFF_W2 + 1920 * 1024;

constexpr u32 L_XROW = 147456, L_TAB = 160768, L_JUNK = 162816;
constexpr u32 KVS = 8256, L_WO = 57856;
constexpr u32 L_S0 = 122880, L_RELAY = L_S0, L_HS = 155648, L_CSP = 157696, L_AUG = 160768;
constexpr u32 L_B1 = 163072, L_B2 = 163328, L_CTRL = 163584;
constexpr u32 C_FREE = L_CTRL, C_X3 = L_CTRL + 4, C_SCR = L_CTRL + 12, C_W1 = L_CTRL + 16, C_X4 = L_CTRL + 20, C_FREE2 = L_CTRL + 36, C_HS = L_CTRL + 40,
              C_AUG = L_CTRL + 48, C_AGG = L_CTRL + 52, C_P1 = L_CTRL + 56, C_WO = L_CTRL + 60, C_V = L_CTRL + 64;
constexpr int VLAG = 2;
constexpr int WOB = 2;
constexpr int W2A = 5, W2B = 12;
constexpr int HF0 = 12;
#define VMW2() VMCNT(7)
constexpr u32 CODE_BYTES = 46080;

template <int N> struct IC { static constexpr int v = N; };
DI rsrc_t mkrsrc(const void* p, u32 bytes) { return __builtin_amdgcn_make_buffer_rsrc((void*)p, (short)0, (int)bytes, 0x00020000); }
DI void glds(rsrc_t rs, u32 voff, u32 soff, u32 l) {
  asm volatile("s_mov_b32 m0, %0\n\ts_nop 0\n\tbuffer_load_dwordx4 %1, %2, %3 offen lds" ::"s"(l), "v"(voff), "s"(rs), "s"(soff) : "memory");
}
DI void glds_nt(rsrc_t rs, u32 voff, u32 soff, u32 l) {
  asm volatile("s_mov_b32 m0, %0\n\ts_nop 0\n\tbuffer_load_dwordx4 %1, %2, %3 offen nt lds" ::"s"(l), "v"(voff), "s"(rs), "s"(soff) : "memory");
}
DI void glds_dev(rsrc_t rs, u32 voff, u32 soff, u32 l) {
  asm volatile("s_mov_b32 m0, %0\n\ts_nop 0\n\tbuffer_load_dwordx4 %1, %2, %3 offen sc1 lds" ::"s"(l), "v"(voff), "s"(rs), "s"(soff) : "memory");
}
DI void glds4(rsrc_t rs, u32 voff, u32 soff, u32 l) {
  asm volatile("s_mov_b32 m0, %0\n\ts_nop 0\n\tbuffer_load_dwordx4 %1, %2, %3 offen sc1 lds\n\tbuffer_load_dwordx4 %1, %2, %3 offen offset:1024 sc1 lds\n\t"
               "buffer_load_dwordx4 %1, %2, %3 offen offset:2048 sc1 lds\n\tbuffer_load_dwordx4 %1, %2, %3 offen offset:3072 sc1 lds" ::"s"(l), "v"(voff), "s"(rs), "s"(soff));
}
DI u32x4 ld_dev(rsrc_t rs, u32 voff, u32 soff) { return __builtin_amdgcn_raw_buffer_load_b128(rs, voff, soff, 16); }
DI u32x4 ld_at(rsrc_t rs, u32 voff, u32 soff, u32 c) { return ld_dev(rs, voff + (c & 4095), soff + (c & ~4095u)); }
DI u32x4 ld_raw(rsrc_t rs, u32 voff, u32 soff) {
  u32x4 r;
  asm volatile("buffer_load_dwordx4 %0, %1, %2, %3 offen sc1" : "=&v"(r) : "v"(voff), "s"(rs), "s"(soff) : "memory");
  return r;
}
DI void st_dev(u32x4 v, rsrc_t rs, u32 voff, u32 soff) {
  asm volatile("buffer_store_dwordx4 %0, %1, %2, %3 offen sc1\n\ts_nop 1" ::"v"(v), "v"(voff), "s"(rs), "s"(soff) : "memory");
}
DI void st_plain(u32x4 v, rsrc_t rs, u32 voff, u32 soff) {
  asm volatile("buffer_store_dwordx4 %0, %1, %2, %3 offen\n\ts_nop 1" ::"v"(v), "v"(voff), "s"(rs), "s"(soff) : "memory");
}
DI u32x4 lds128(u32 a) { return *(LDS u32x4*)(size_t)a; }
DI f32x4 ldsf4(u32 a) { return *(LDS f32x4*)(size_t)a; }
DI void lds_st4(u32 a, f32x4 v) { *(LDS f32x4*)(size_t)a = v; }
DI u32x4 ldstr(u32 a0, u32 a1) {
  u32x2 lo = __builtin_bit_cast(u32x2, __builtin_amdgcn_ds_read_tr16_b64_v4i16((LDS s16x4*)(size_t)a0));
  u32x2 hi = __builtin_bit_cast(u32x2, __builtin_amdgcn_ds_read_tr16_b64_v4i16((LDS s16x4*)(size_t)a1));
  return u32x4{lo.x, lo.y, hi.x, hi.y};
}
DI f32x16 mfma32(u32x4 a, u32x4 b, f32x16 c) { return __builtin_amdgcn_mfma_f32_32x32x16_bf16(__builtin_bit_cast(bf16x8, a), __builtin_bit_cast(bf16x8, b), c, 0, 0, 0); }
DI f32x4 mfma16(u32x4 a, u32x4 b, f32x4 c) { return __builtin_amdgcn_mfma_f32_16x16x32_bf16(__builtin_bit_cast(bf16x8, a), __builtin_bit_cast(bf16x8, b), c, 0, 0, 0); }
DI float dot2(u32 a, u32 b, float c) { return __builtin_amdgcn_fdot2_f32_bf16(__builtin_bit_cast(bf16x2, a), __builtin_bit_cast(bf16x2, b), c, false); }
DI u32 pk(float a, float b) { f32x2 v = {a, b}; return __builtin_bit_cast(u32, __builtin_convertvector(v, bf16x2)); }
DI float bflo(u32 v) { return __uint_as_float(v << 16); }
DI float bfhi(u32 v) { return __uint_as_float(v & 0xffff0000u); }
DI float halves_sum(float x) { auto r = __builtin_amdgcn_permlane32_swap(__float_as_uint(x), __float_as_uint(x), false, false); return __uint_as_float(r[0]) + __uint_as_float(r[1]); }
DI u32x4 widen32(u32 a0, u32 a1, u32 b0, u32 b1) {
  auto r0 = __builtin_amdgcn_permlane32_swap(a0, b0, false, false);
  auto r1 = __builtin_amdgcn_permlane32_swap(a1, b1, false, false);
  return u32x4{r0[0], r1[0], r0[1], r1[1]};
}
DI u32x4 widen16(u32 a0, u32 a1, u32 b0, u32 b1) {
  auto r0 = __builtin_amdgcn_permlane16_swap(a0, b0, false, false);
  auto r1 = __builtin_amdgcn_permlane16_swap(a1, b1, false, false);
  return u32x4{r0[0], r1[0], r0[1], r1[1]};
}
#define VMCNT(n) asm volatile("s_waitcnt vmcnt(" #n ")" ::: "memory")
#define SB() __builtin_amdgcn_sched_barrier(0)
#define OOB 0x7ffffff0u
#define BAR() do { asm volatile("s_waitcnt lgkmcnt(0)" ::: "memory"); __builtin_amdgcn_s_barrier(); asm volatile("" ::: "memory"); } while (0)
#define DRAIN() do { __builtin_amdgcn_s_waitcnt(0x0F70); asm volatile("s_waitcnt vmcnt(0)" ::: "memory"); } while (0)
DI u32 opaque(u32 v) { asm volatile("" : "+v"(v)); return v; }
DI u32 sopaque(u32 v) { asm volatile("" : "+s"(v)); return v; }
DI void lds_inc(u32 a) { asm volatile("s_mov_b64 exec, 1\n\tds_add_u32 %0, %1\n\ts_mov_b64 exec, -1" ::"v"(a), "v"(1u) : "memory"); }
DI u32 lds_ticket(u32 a) {
  u32 r;
  asm volatile("s_mov_b64 exec, 1\n\tds_add_rtn_u32 %0, %1, %2\n\ts_mov_b64 exec, -1\n\ts_waitcnt lgkmcnt(0)" : "=&v"(r) : "v"(a), "v"(1u) : "memory");
  return __builtin_amdgcn_readfirstlane(r);
}
DI void set_flag(rsrc_t rws, u32 off, u32 tag) {
  asm volatile("s_mov_b64 exec, 1\n\tbuffer_store_dword %0, off, %1, %2 sc1\n\ts_mov_b64 exec, -1" ::"v"(tag), "s"(rws), "s"(off) : "memory");
}
DI void lds_wait(u32 a, u32 target) {
  u32 t, n;
  asm volatile("s_mov_b32 %1, 0x100000\n"
               "1:\n\tds_read_b32 %0, %2\n\ts_waitcnt lgkmcnt(0)\n\tv_cmp_le_u32 vcc, %3, %0\n\ts_cbranch_vccnz 2f\n\ts_sleep 1\n\t"
               "s_sub_u32 %1, %1, 1\n\ts_cbranch_scc0 1b\n"
               "2:" : "=&v"(t), "=&s"(n) : "v"(a), "s"(target) : "vcc", "scc", "memory");
}
DI void give_up(rsrc_t rws) { __builtin_amdgcn_raw_buffer_store_b32(0xdeadu, rws, OFF_ERR, 0, 16); }
DI void post(rsrc_t rws, u32 voff, u32 soff, u32 tag) {
  asm volatile("buffer_store_dword %0, %1, %2, %3 offen sc1" ::"v"(tag), "v"(voff), "s"(rws), "s"(soff) : "memory");
}
DI void await(rsrc_t rws, u32 v4, u32 soff, u32 tag, u64 mask) {
  u32 f, n;
  asm volatile("s_mov_b32 %1, 0x10000\n\ts_mov_b64 exec, %6\n"
               "1:\n\tv_mov_b32 %0, 0\n\tbuffer_atomic_or %0, %2, %3, %4 offen sc0 sc1\n\ts_waitcnt vmcnt(0)\n\tv_cmp_ne_u32 vcc, %5, %0\n\ts_cbranch_vccz 2f\n\t"
               "s_sub_u32 %1, %1, 1\n\ts_cbranch_scc0 1b\n"
               "2:\n\ts_mov_b64 exec, -1" : "=&v"(f), "=&s"(n) : "v"(v4), "s"(rws), "s"(soff), "s"(tag), "s"(mask) : "vcc", "scc", "memory");
  __builtin_amdgcn_s_waitcnt(0x0F70);
}
DI void peek(u32& r, rsrc_t rws, u32 v4, u32 soff, u64 mask) {
  asm volatile("s_mov_b64 exec, %4\n\tv_mov_b32 %0, 0\n\tbuffer_atomic_or %0, %1, %2, %3 offen sc0 sc1\n\ts_mov_b64 exec, -1" : "+v"(r) : "v"(v4), "s"(rws), "s"(soff), "s"(mask) : "memory");
}
template <int CTRL> DI float dpp(float x) { return __builtin_bit_cast(float, __builtin_amdgcn_mov_dpp(__builtin_bit_cast(int, x), CTRL, 0xf, 0xf, true)); }
DI float half_wave_sum(float x) {
  x += dpp<0xB1>(x); x += dpp<0x4E>(x); x += dpp<0x141>(x); x += dpp<0x128>(x);
  auto r = __builtin_amdgcn_permlane16_swap(__float_as_uint(x), __float_as_uint(x), false, false);
  return __uint_as_float(r[0]) + __uint_as_float(r[1]);
}

__global__ __launch_bounds__(512, 2) void fused_block(const void* __restrict__ px, const void* __restrict__ pwqkv, const void* __restrict__ pwo,
                                                      const void* __restrict__ pw1, const void* __restrict__ pb1, const void* __restrict__ pw2,
                                                      const void* __restrict__ pb2, void* __restrict__ pout, void* pws) {
  __shared__ __attribute__((aligned(16))) char smem[163840];
  const u32 tid = threadIdx.x, lane = tid & 63;
  const u32 w = __builtin_amdgcn_readfirstlane(tid >> 6);
  u32 b = blockIdx.x;
  u64 pc0;
  asm volatile("s_getpc_b64 %0" : "=s"(pc0));
  pc0 &= ~127ull;
  const rsrc_t rcode = mkrsrc((const void*)pc0, CODE_BYTES);
  asm volatile("s_mov_b32 m0, %0\n\ts_nop 0\n\tbuffer_load_dword %1, %2, 0 offen lds" ::"s"(L_JUNK), "v"(lane * 32768 + ((b & ~7u) + w) * 128), "s"(rcode) : "memory");
  const u32 hh = lane >> 5, l31 = lane & 31, l15 = lane & 15, g4 = lane >> 4, q4 = (lane & 15) >> 2, p4 = lane & 3, blk = (lane >> 4) & 1;
  asm volatile("" ::"s"((u32)(size_t)(LDS char*)smem));
  const u32 v16 = lane * 16, v4 = lane * 4;

  const bool spare = (b & 1) && b >= 224;
  const u32 rl = ((b >> 3) * (32 + 5 * (b & 1))) >> 8, cl = (b >> 3) - rl * (8 - (b & 1)), cb = 8 * (b & 1) + cl, rb = 2 * (b & 6) + rl;
  const u32 voffA = opaque((8 * (w & 3) + (lane >> 3)) * 1280 + (((lane & 7) ^ ((4 * w + g4) & 7)) << 4));
  const u32 voffB = opaque((4 * (w & 3) + g4) * 3840 + ((l15 ^ ((g4 << 2) | (w & 3))) << 4));
  u32 sk = ((cl * 5) >> 2) + ((rl * 5) >> 1) + ((b >> 1) & 3);
  sk = sk >= 10 ? sk - 10 : sk;
  const u32 sk0 = sk;
  u32 sa = 0, sb = 0;
  auto next = [&]() { sa = rb * 163840 + sk * 128; sb = cb * 256 + sk * 245760; sk = sk == 9 ? 0 : sk + 1; };
  next();
  asm volatile("" : "+s"(sa), "+s"(sb));
  asm volatile("" : "+s"(px), "+s"(pwqkv), "+s"(pwo), "+s"(pw1), "+s"(pb1), "+s"(pw2), "+s"(pb2), "+s"(pout), "+s"(pws));
  const rsrc_t rx = mkrsrc(px, 2048 * 1280), rw = mkrsrc(pwqkv, 640 * 3840);
  auto piece = [&](int buf, u32 k) {
    const u32 l = buf * 32768 + (w & 3) * 1024 + k * 4096;
    glds(rx, voffA, sa + k * 40960, l);
    glds(rw, voffB, sb + k * 61440, l + 16384);
  };
  const u32 xrow = 256 * (b & 7) + (b & ~7u) + 2 * (w & 3);
  auto row_loads = [&]() {
    glds(rx, v16, xrow * 1280, L_XROW + (w & 3) * 3072);
    glds(rx, v16, xrow * 1280 + 1024, L_XROW + (w & 3) * 3072 + 1024);
    glds(rx, hh ? OOB : v16, xrow * 1280 + 2048, L_XROW + (w & 3) * 3072 + 2048);
  };
  const rsrc_t rws = mkrsrc(pws, WS_END);
  u32 tag, epoch;
  asm volatile("buffer_load_dword %0, off, %1, 0 sc1" : "=&v"(epoch) : "s"(rws) : "memory");
  if (__builtin_expect(!spare, 1)) {
    if (w >= 4) row_loads();
    piece(0, (w >> 2) * 2); piece(0, (w >> 2) * 2 + 1);
  }
  b = sopaque(b);
  const u32 xcd = b & 7, j = b >> 3, pr = xcd >> 1, xo = xcd & 1;
  const u32 cb3 = 4 * xo + (j & 3), g3 = 16 * pr + 8 * (j >> 4) + ((j >> 2) & 3);
  const u32 cb4 = 8 * xo + (j & 7);
  const u32 half = w >> 2, w4 = w & 3, gr = g3 + 4 * half;
  const u32 head = 4 * xcd + (j & 3), c = j >> 2, odd = head & 1;
  const u32 pb = 5 * (head >> 1), plA = pb + 3 * odd, plC = pb + 2;
  const u32 kh = w & 1, g4r = 16 * pr + 8 * (j >> 4) + 4 * half + 2 * ((j >> 3) & 1) + ((w >> 1) & 1);
  const u32 mb1 = OFF_MB1 + (c * 32 + head) * 128, mb = OFF_MB + (b * 8 + w) * 128;
  auto wg35 = [&](u32 g) { return OFF_MB + ((g & 8) * 16 + (g & 3) * 32 + (g >> 4) * 2) * 1024; };
  const u32 selv = (((l15 >> 2) ^ g4) & 1) ? 0u : 0x3f803f80u;
  const u32x4 sel = {selv, selv, selv, selv};
  u32 post1 = OOB;
  auto side_loads = [&]() {
    if (w < 3)
      asm volatile("s_mov_b64 exec, 1\n\tbuffer_atomic_or %0, off, %1, %2 sc1\n\ts_mov_b64 exec, -1" ::"v"(0u), "s"(rws),
                   "s"(w == 0 ? mb1 : w == 1 ? OFF_MB0 + b * 128 : OFF_MBW + b * 128) : "memory");
    if (w == 5 || w == 6)
      asm volatile("s_mov_b32 m0, %0\n\ts_nop 0\n\tbuffer_load_dword %1, %2, %3 offen lds" ::"s"(w == 5 ? L_B1 : L_B2), "v"(lane < (w == 5 ? 48u : 40u) ? v4 : OOB),
                   "s"(mkrsrc(w == 5 ? pb1 : pb2, w == 5 ? 3072 : 1280)), "s"(w == 5 ? cb4 * 192 : cb3 * 160) : "memory");
  };
  auto get_tag = [&]() {
    asm volatile("v_readfirstlane_b32 %0, %1\n\ts_nop 3\n\ts_mul_i32 %0, %0, 0x9e3779b1\n\ts_add_u32 %0, %0, 0x7f4a7c15" : "=&s"(tag) : "v"(epoch) : "scc", "memory");
  };
  auto row_stats = [&](bool drain_first) {
    f32x2 v[10], sum = {0.f, 0.f};
    const u32 a = opaque(L_XROW + w4 * 3072 + hh * 1280 + l31 * 8);
#pragma unroll
    for (int k = 0; k < 5; ++k) {
      const u32x2 d = *(LDS u32x2*)(size_t)(a + k * 256);
      v[2 * k] = f32x2{bflo(d.x), bfhi(d.x)}; v[2 * k + 1] = f32x2{bflo(d.y), bfhi(d.y)};
      sum += v[2 * k] + v[2 * k + 1];
    }
    const float mu = half_wave_sum(sum.x + sum.y) * (1.0f / 640.0f);
    f32x2 sq = {0.f, 0.f};
#pragma unroll
    for (int k = 0; k < 10; ++k) sq += (v[k] - mu) * (v[k] - mu);
    const float rstd = __builtin_amdgcn_rsqf(half_wave_sum(sq.x + sq.y) * (1.0f / 640.0f) + 1e-5f);
    const float m1 = bflo(pk(-mu, 0.f));
    u32x4 gv = {pk(m1, -mu - m1), tag, __float_as_uint(rstd), tag};
    if (drain_first) asm volatile("s_waitcnt vmcnt(0)" : "+v"(gv)::"memory");
    st_dev(gv, rws, l31 == 0 ? hh * 16 : OOB, OFF_XS + xrow * 16);
  };
  if (__builtin_expect(!spare, 1)) {
    side_loads();
    if (tid < 32) *(LDS u32*)(size_t)(L_CTRL + tid * 4) = 0;
    next();
    if (!half) { piece(1, 0); piece(1, 1); }
    auto wfa = [&](u32 ctile) { return 16384 + (8 * hh + q4) * 256 + (((4 * ctile + 2 * blk + (p4 >> 1)) ^ ((q4 << 2) | (2 * hh))) << 4) + 8 * (p4 & 1); };
    if (!half) {
      const u32 wm = w >> 1, wn = w & 1;
      const u32 ax0 = (64 * wm + l31) * 128 + ((hh ^ ((l31 >> 1) & 7)) << 4);
      u32 ax[2][4], aw[2][2][2];
#pragma unroll
      for (int i = 0; i < 4; ++i) { ax[0][i] = opaque(ax0 ^ (i << 5)); ax[1][i] = opaque((ax0 ^ (i << 5)) + 65536); }
#pragma unroll
      for (int n = 0; n < 4; ++n) { const u32 a = wfa(2 * wn + (n & 1)) + (n >> 1) * 65536; aw[n >> 1][n & 1][0] = opaque(a); aw[n >> 1][n & 1][1] = opaque((a ^ 16) + 1024); }
      const u32 so1 = sopaque(OFF_QKV + (4 * rb + 2 * wm) * GS_QKV + (16 * cb + 8 * wn) * 512);
      const u32 tca = opaque(L_TAB + (64 * wn + l31) * 8), txa = opaque(L_TAB + 1024 + (64 * wm + l31) * 8);
      {
        const u32 ty = (cb * 205) >> 10, cbl = cb - 5 * ty, hd = ((cbl * 32 * 205) >> 10) + (lane & 7), first = (5 * hd) >> 1, nx = (lane >> 3) & 1, cc = (rb >> 1) + nx;
        const bool val = lane < 16 && hd < 32 && first < 16 * cbl + 16 && cc < 8 && (ty > 0 || nx == 0);
        post1 = opaque(val ? (cc * 32 + hd) * 128 + (ty * 8 + ((rb & 1) + 2 - 2 * nx) * 2 + (cbl - (first >> 4))) * 4 : OOB);
      }
      f32x16 acc[2][2] = {};
      u32x4 fx[2][4][2], fw[2][4][2];
      u32x2 tc[2], tx[2];
      auto round = [&](auto IT) __attribute__((always_inline)) {
        constexpr int it = decltype(IT)::v;
        constexpr int cu = it & 1, pv = cu ^ 1, hi = (it >> 1) & 1;
        const u32 bo = cu * 32768;
        if (it == 0) { VMCNT(4); get_tag(); }
        else VMCNT(2);
        if (it < 8) next();
        BAR();
        SB();
#pragma unroll
        for (int q = 0; q < 16; ++q) {
          const int i = q >> 2, n = q & 3, mt = (n ^ (n >> 1)) & 1, ct = n >> 1;
          if (it > 0) acc[mt][ct] = mfma32(fw[pv][i][ct], fx[pv][i][mt], acc[mt][ct]);
          SB();
          if (q < 12) {
            const int ri = q / 3, rn = q % 3;
            if (rn < 2) fw[cu][ri][rn] = ldstr(aw[hi][rn][0] + bo + ri * 4096, aw[hi][rn][1] + bo + ri * 4096);
            else { fx[cu][ri][0] = lds128(ax[hi][ri] + bo); fx[cu][ri][1] = lds128(ax[hi][ri] + bo + 4096); }
          }
          if (q == 12 && it < 8) piece((it + 2) & 3, 3);
          SB();
        }
      };
      round(IC<0>{}); round(IC<1>{}); round(IC<2>{}); round(IC<3>{}); round(IC<4>{});
      round(IC<5>{}); round(IC<6>{}); round(IC<7>{}); round(IC<8>{});
      VMCNT(0);
      BAR();
      tc[0] = *(LDS u32x2*)(size_t)tca; tx[0] = *(LDS u32x2*)(size_t)txa; tc[1] = *(LDS u32x2*)(size_t)(tca + 256); tx[1] = *(LDS u32x2*)(size_t)(txa + 256);
      u32 d[8];
      auto finish = [&](int n, int g) __attribute__((always_inline)) {
        const int mt = n >> 1, ct = n & 1;
        const float rs = __uint_as_float(tx[mt].y);
        if (g == 0 || g == 1 || g == 3 || g == 4) {
          const int u0 = g < 2 ? 2 * g : 2 * g - 2;
#pragma unroll
          for (int u = u0; u < u0 + 2; ++u) { d[u] = pk(acc[mt][ct][2 * u] * rs, acc[mt][ct][2 * u + 1] * rs); asm volatile("" : "+v"(d[u])); }
        }
        if (g == 2) st_dev(widen32(d[0], d[1], d[2], d[3]), rws, v16, so1 + mt * GS_QKV + ct * 2048);
        if (g == 5) st_dev(widen32(d[4], d[5], d[6], d[7]), rws, v16, so1 + mt * GS_QKV + ct * 2048 + 1024);
      };
#pragma unroll
      for (int n = 0; n < 4; ++n) {
        const int mt = n >> 1, ct = n & 1;
#pragma unroll
        for (int q = 0; q < 9; ++q) {
          SB();
          if (q == 4) {
            const u32 m = hh ? 0u : tx[mt].x;
            acc[mt][ct] = mfma32(u32x4{tc[ct].x, tc[ct].y, 0u, 0u}, u32x4{m, m, 0u, 0u}, acc[mt][ct]);
          } else {
            acc[mt][ct] = q < 4 ? mfma32(fw[0][q][ct], fx[0][q][mt], acc[mt][ct]) : mfma32(fw[1][q - 5][ct], fx[1][q - 5][mt], acc[mt][ct]);
          }
          SB();
          if (n == 0 && q < 8) fw[1][q & 3][q >> 2] = ldstr(aw[0][q >> 2][0] + 32768 + (q & 3) * 4096, aw[0][q >> 2][1] + 32768 + (q & 3) * 4096);
          if (n == 0 && q < 8) fx[1][q & 3][q >> 2] = lds128(ax[0][q & 3] + 32768 + (q >> 2) * 4096);
          if (n > 0 && q >= 2 && q < 8) finish(n - 1, q - 2);
        }
      }
      SB();
#pragma unroll
      for (int g = 0; g < 6; ++g) finish(3, g);
    } else {
      auto stage = [&](int buf) { next(); piece(buf, 0); piece(buf, 1); piece(buf, 2); };
      auto wo_block = [&](u32 k) { glds4(rws, v16, OFF_WO + cb3 * 102400 + k * 4096, L_WO + k * 4096); };
      __builtin_amdgcn_s_setprio(1);
      VMCNT(4);
      get_tag();
      row_stats(false);
      piece(1, 2); piece(1, 3);
      VMCNT(5);
      BAR();
      stage(2);
      f32x4 cs4 = {};
      {
        const u32 a = wfa(w4);
#pragma unroll
        for (int i = 0; i < 4; ++i) cs4 = mfma16(sel, ldstr(a + i * 4096, (a ^ 16) + 1024 + i * 4096), cs4);
        const u32x2 gq = {__float_as_uint(cs4[0] + 0.f), tag};
        asm volatile("buffer_store_dwordx2 %0, %1, %2, %3 offen sc1" ::"v"(gq), "v"(hh ? OOB : lane * 8), "s"(rws), "s"(OFF_CSQ + (cb * 10 + sk0) * 1024 + w4 * 256) : "memory");
      }
      VMCNT(7); BAR(); stage(3);
      VMCNT(7); BAR(); stage(0);
      VMCNT(6); BAR(); stage(1);
      u32x4 xs;
      u32x2 cg[5];
      auto fetch = [&](int part) {
        if (part == 0) xs = ld_dev(rws, l31 * 16, OFF_XS + (128 * rb + 32 * w4) * 16);
#pragma unroll
        for (int k = part ? 2 : 0; k < (part ? 5 : 2); ++k) cg[k] = __builtin_amdgcn_raw_buffer_load_b64(rws, hh * 5120 + l31 * 8, OFF_CSQ + (cb * 10 + k) * 1024 + w4 * 256, 16);
      };
      auto stale = [&]() {
        u32 t = (xs.y ^ tag) | (xs.w ^ tag);
#pragma unroll
        for (int k = 0; k < 5; ++k) t |= cg[k].y ^ tag;
        return __any(t != 0);
      };
      VMCNT(6); BAR(); stage(2);
      VMCNT(6); BAR(); stage(3);
      VMCNT(6); BAR(); stage(0);
      VMCNT(6); BAR(); stage(1);
      u32 pw = tag;
      peek(pw, rws, v4, OFF_MBW + b * 128, 0xffffull);
      fetch(0); fetch(1);
      VMCNT(13); BAR();
      asm volatile("s_waitcnt vmcnt(0)" : "+v"(pw)::"memory");
      if (__builtin_expect(stale(), 0)) {
        u32 n = 0;
#pragma unroll 1
        do {
          __builtin_amdgcn_s_sleep(8);
          fetch(0); fetch(1);
          __builtin_amdgcn_s_waitcnt(0x0F70);
          asm volatile("" ::: "memory");
        } while (stale() && ++n < 65536);
        if (n >= 65536) give_up(rws);
      }
      {
        const float cs = halves_sum(((__uint_as_float(cg[0].x) + __uint_as_float(cg[1].x)) + (__uint_as_float(cg[2].x) + __uint_as_float(cg[3].x))) + __uint_as_float(cg[4].x));
        const u32 c11 = pk(cs, cs);
        const float c2 = cs - bflo(c11);
        const float qs = cb < 5 ? 0.22360679774997896f * 1.4426950408889634f : 1.0f;
        *(LDS u32x2*)(size_t)(L_TAB + hh * 1024 + (32 * w4 + l31) * 8) = hh ? u32x2{xs.x, __float_as_uint(__uint_as_float(xs.z) * qs)} : u32x2{c11, pk(c2, c2)};
      }
      BAR();
      if (!__all(pw == tag)) await(rws, v4, OFF_MBW + b * 128, tag, 0xffffull);
#pragma unroll
      for (int k = 0; k < WOB; ++k) wo_block(2 + 4 * k + w4);
      __builtin_amdgcn_s_setprio(0);
    }
  } else {
    const u32 sidx = pr * 4 + (j - 28);
    const rsrc_t rr = mkrsrc(pwo, 640 * 1280);
    if (w == 4) {
      u64 pc;
      asm volatile("s_getpc_b64 %0" : "=s"(pc));
      asm volatile("s_mov_b32 m0, %0\n\ts_nop 0\n\tbuffer_load_dword %1, %2, 0 offen lds" ::"s"(L_JUNK), "v"(lane < 24 ? (((u32)pc - (u32)pc0) & ~127u) + lane * 128 : OOB), "s"(rcode) : "memory");
    }
    const u32 vo = (lane >> 3) * 1280 + (lane & 7) * 16;
    auto job = [&](int k) { return k ? 128 + w * 16 + sidx : sidx * 8 + w; };
#pragma unroll
    for (int k = 0; k < 2; ++k) {
      const u32 u = job(k), kb = (u * 6554) >> 16, nb = u - kb * 10;
#pragma unroll
      for (int i = 0; i < 4; ++i) {
        const u32 kq = (kb * 205) >> 10, si = kb - 5 * kq, pl = 20 * kq + (si < 4 ? 5 * si + i + (i >> 1) : 5 * i + 2);
        glds_nt(rr, u < 200 ? vo : OOB, pl * 10240 + nb * 128, w * 8192 + k * 4096 + i * 1024);
      }
      if (k == 0) __builtin_amdgcn_s_barrier();
    }
    side_loads();
    if (half) row_loads();
    if (tid < 32) *(LDS u32*)(size_t)(L_CTRL + tid * 4) = 0;
    const u32 ta = w * 8192 + (8 * g4 + q4) * 128 + 8 * p4;
#pragma unroll
    for (int k = 0; k < 2; ++k) {
      const u32 u = job(k), kb = (u * 6554) >> 16, nb = u - kb * 10;
      u32x4 v[4];
      VMCNT(4);
#pragma unroll
      for (int f = 0; f < 4; ++f) v[f] = ldstr(ta + k * 4096 + 32 * f, ta + k * 4096 + 32 * f + 512);
#pragma unroll
      for (int f = 0; f < 4; ++f) {
        const u32 ctile = 4 * nb + f, cbk = (ctile * 205) >> 10, ct = ctile - cbk * 5;
        st_dev(v[f], rws, u < 200 ? v16 : OOB, OFF_WO + ((cbk * 20 + kb) * 5 + ct) * 1024);
      }
    }
    VMCNT(8);
    get_tag();
    if (half) row_stats(true); else VMCNT(0);
    BAR();
    if (w < 4) post(rws, lane * 128, OFF_MBW + w * 8192 + sidx * 4, tag);
    await(rws, v4, OFF_MBW + b * 128, tag, 0xffffull);
#pragma unroll 1
    for (u32 k = 2 + w; k < 2 + 4 * WOB; k += 8) glds4(rws, v16, OFF_WO + cb3 * 102400 + k * 4096, L_WO + k * 4096);
  }
  const u32 vkv = opaque(hh * GS_QKV + l31 * 16), vlo = opaque(hh ? OOB : v16);
  const u32 sq = sopaque(OFF_QKV + (8 * c + w) * GS_QKV), skv = sopaque(OFF_QKV + ((c == 0 && w < 4) ? 2 * w : 8 * c - 8 + 2 * w) * GS_QKV);
  const u32 pA = sopaque(plA * 512), pC = sopaque(plC * 512);
  u64 need1;
  {
    const u32 first = pb + 2 * odd, rbi = (lane >> 1) & 3, rkv = (w >> 1) | (c ? 0 : 2);
    need1 = __ballot(lane < 24 && ((lane >= 8 && rbi == rkv) || (lane < 16 && rbi == 2 + half)) && (!(lane & 1) || ((first + 2) >> 4) != (first >> 4)));
    asm volatile("" : "+s"(need1));
  }
  const u32 ka = opaque(hh * KVS + (32 * w + l31) * 16), kb = opaque((2 + 4 * hh) * KVS + (32 * w + l31) * 16);
  const u32 va = opaque((blk ? 5 + (p4 & 1) : 3 + (p4 >> 1)) * KVS + 8 * (blk ? odd & ~p4 : p4 & 1) + (32 * w + 4 * hh + q4) * 16);
  const u32 rF = (pr * 8 + (j >> 2)) * 8 + 2 * w4;
  const u32 rlv = opaque(rF < 240 ? (lane >> 1) * 1280 + (lane & 1) * 16 : OOB), rsv = opaque(rF < 240 ? v16 : OOB);
  const u32 rla = opaque(L_RELAY + w4 * 2048 + (8 * g4 + q4) * 32 + 8 * p4);
  u32 rlo[2];
#pragma unroll
  for (int f = 0; f < 2; ++f) { const u32 F = rF + f, k2 = (F * 205) >> 10; rlo[f] = sopaque(k2 * 40960 + cb3 * 160 + (F - 5 * k2) * 32); }
  auto relay_load = [&](int f) { glds_nt(mkrsrc(pw2, 1536 * 1280), rlv, rlo[f], L_RELAY + w4 * 2048 + f * 1024); };
  const rsrc_t rw1 = mkrsrc(pw1, 640 * 3072);
  u32 w1v[3];
#pragma unroll
  for (int i = 0; i < 3; ++i) { const u32 ch = 64 * i + lane, r = (ch * 5462) >> 16; w1v[i] = opaque(r * 3072 + (ch - r * 12) * 16 + cb4 * 192); }
  auto w1s = [&](int n) {
    const u32 q = (n < 2 ? 4 * n + 12 * half : n < 4 ? 4 * n - 8 + 12 * (1 - half) : 8) + w4 + 5 * (j >> 3);
    return (q >= 20 ? q - 20 : q) + ((n < 2 || (n == 4 && !half)) ? 0 : 20);
  };
  u32 w1n[5];
#pragma unroll
  for (int n = 0; n < 5; ++n) w1n[n] = sopaque(w1s(n) * 3072);
  auto w1step = [&](u32 s) {
#pragma unroll
    for (int i = 0; i < 3; ++i) glds(rw1, w1v[i], s * 16, s + i * 1024);
  };
  const u32 xv = opaque(l15 * 1280 + g4 * 8), xso = sopaque(gr * 40960 + 160 * cb3 + 32 * w4);
  u32 woa[5];
#pragma unroll
  for (int cc = 0; cc < 5; ++cc) woa[cc] = opaque(L_WO + w4 * 25600 + (cc < 4 ? ((w4 + cc) & 3) : 4) * 1024 + v16);
  if (!half) {
    DRAIN();
    if (lds_ticket(C_P1) == 3) post(rws, post1, OFF_MB1, tag);
  }
  f32x16 c0, c8;
  auto masks = [&]() {
    const int e = (int)opaque(l31 - 4 * hh);
#pragma unroll
    for (int r = 0; r < 16; ++r) { const bool cnd = 8 * (r >> 2) + (r & 3) > e; c0[r] = cnd ? 0.f : -1e30f; c8[r] = cnd ? -1e30f : 0.f; }
    asm volatile("" : "+v"(c0), "+v"(c8));
  };
  masks();
  lds_wait(C_P1, 4);
  if (half) {
#pragma unroll 1
    for (u32 k = 2 + 4 * WOB + w4; k < 27; k += 4) { const u32 kk = k < 25 ? k : k - 25; glds4(rws, v16, OFF_WO + cb3 * 102400 + kk * 4096, L_WO + kk * 4096); }
  }
  u32x2 xr[2][2] = {};
  *(LDS u32x4*)(size_t)(6 * KVS + tid * 16) = u32x4{0x3f803f80u, 0x3f80u, (c == 0 && tid < 256) ? 0xf149u : 0u, 0u};
  await(rws, v4, mb1, tag, need1);
  u32x4 q1 = ld_raw(rws, v16, sq + pA), q2 = ld_raw(rws, vlo, sq + pC);
  u32x4 k1 = ld_raw(rws, v16, sq + pA + 40960), k2 = ld_raw(rws, vlo, sq + pC + 40960);
  glds_dev(rws, vkv, skv + pA + 40960, w * 1024);
  glds_dev(rws, vkv, skv + pA + 41472, w * 1024 + KVS);
  glds_dev(rws, vkv, skv + pC + 40960, w * 1024 + 2 * KVS);
  lds_inc(C_WO);
  u32x4 wf[5][5];
  lds_wait(C_WO, 8);
#pragma unroll
  for (int cc = 0; cc < 5; ++cc)
#pragma unroll
    for (int si = 0; si < 5; ++si) wf[si][cc] = lds128(woa[cc] + si * 5120);
  SB();
  glds_dev(rws, vkv, skv + pA + 81920, w * 1024 + 3 * KVS);
  glds_dev(rws, vkv, skv + pA + 82432, w * 1024 + 4 * KVS);
  glds_dev(rws, vkv, skv + pC + 81920, w * 1024 + 5 * KVS);
  if (half) {
    VMCNT(3);
    BAR();
    VMCNT(0);
    lds_inc(C_V);
    if (w == 7) set_flag(rws, OFF_SEEN + b * 4, tag);
  }
  asm volatile("s_waitcnt vmcnt(6)" : "+v"(q1), "+v"(q2), "+v"(k1), "+v"(k2)::"memory");
  if (odd) { q2.x = 0u; q2.y = 0u; } else { q2.z = 0u; q2.w = 0u; }
  float shift;
  {
    float d = dot2(k1.x, q1.x, 0.f), d2 = dot2(k2.x, q2.x, 0.f);
    d = dot2(k1.y, q1.y, d); d2 = dot2(k2.y, q2.y, d2);
    d = dot2(k1.z, q1.z, d); d2 = dot2(k2.z, q2.z, d2);
    d = dot2(k1.w, q1.w, d); d2 = dot2(k2.w, q2.w, d2);
    shift = halves_sum(d + d2);
  }
  q2.z |= hh ? 0x3f80u : 0u;
  k2.x |= hh ? 0x3f803f80u : 0u;
  k2.y |= hh ? 0x3f80u : 0u;
  u32x4 w2f[6][5];
  auto rstep = [&](int si) { return (0x543210543210ull >> (4 * (si + (((cb3 & 3) * 3) >> 1)))) & 7; };
  auto ld_w2 = [&](int n) { w2f[n / 5][n % 5] = ld_at(rws, v16, OFF_W2 + (cb3 * 48 + 6 * w + rstep(n / 5)) * 5120, (n % 5) * 1024); };
  u32x4 ow;
  u32x2 oc;
  const f32x16 z = {};
  auto softmax_pv = [&](auto AT) __attribute__((always_inline)) {
    constexpr int attempt = decltype(AT)::v;
    u32x4 q3 = q2;
    {
      const float h1 = bflo(pk(-shift, 0.f)), r1 = -shift - h1, h2 = bflo(pk(r1, 0.f));
      q3.x = hh ? pk(h1, h2) : q2.x;
      q3.y = hh ? pk(r1 - h2, 0.f) : q2.y;
    }
    f32x16 sc = mfma32(k1, q1, c8), sn, o = z;
    sc = mfma32(k2, q3, sc);
    if (attempt == 0 && !half) {
      SB();
      VMCNT(3);
      BAR();
      __builtin_amdgcn_s_setprio(1);
      relay_load(0); relay_load(1);
    }
    constexpr int LAG = attempt == 0 ? VLAG : 0;
    u32x4 ph[VLAG][2], vh[2];
    u32x4 ka1 = lds128(ka), ka2 = lds128(kb), vf[2];
#pragma unroll
    for (int i = 0; i < 9; ++i) {
      const int t = i == 0 ? 8 : i - 1;
      float p[8];
      SB();
      if (LAG && i == LAG) {
        if (!half) { VMCNT(2); lds_inc(C_V); }
        lds_wait(C_V, 8);
      }
      if (i >= LAG) {
        vf[0] = ldstr(va + t * 512, va + t * 512 + 128);
        vf[1] = ldstr(va + t * 512 + 256, va + t * 512 + 384);
      }
      if (i >= LAG && i < 2 * LAG) {
        const int t2 = i == LAG ? 8 : i - LAG - 1;
        vh[0] = ldstr(va + t2 * 512, va + t2 * 512 + 128);
        vh[1] = ldstr(va + t2 * 512 + 256, va + t2 * 512 + 384);
      }
      if (attempt == 0) {
        if (i == 0 && half) w1step(w1n[4]);
        if (i == 3) w1step(w1n[2]);
        if (i == 5) w1step(w1n[3]);
        if (i == 6 && !half) {
          VMCNT(6);
          const u32x4 r2 = ldstr(rla, rla + 128), r3 = ldstr(rla + 1024, rla + 1152);
          st_dev(r2, rws, rsv, OFF_W2 + (cb3 * 240 + rF) * 1024);
          st_dev(r3, rws, rsv, OFF_W2 + (cb3 * 240 + rF + 1) * 1024);
        }
      }
      if (i == 7 && attempt == 0) {
        if (w4 == 0) {
#pragma unroll
          for (int m2 = 0; m2 < 2; ++m2) xr[1][m2] = __builtin_amdgcn_raw_buffer_load_b64(rx, xv, xso + m2 * 20480 + 128, 0);
        }
#pragma unroll
        for (int m2 = 0; m2 < 2; ++m2) xr[0][m2] = __builtin_amdgcn_raw_buffer_load_b64(rx, xv, xso + m2 * 20480, 0);
      }
#pragma unroll
      for (int u = 0; u < 2; ++u) {
        SB();
#pragma unroll
        for (int r = 0; r < 4; ++r) p[r] = __builtin_amdgcn_exp2f(sc[8 * u + r]);
        SB();
        if (i < 8) {
          if (u == 0) sn = mfma32(ka1, q1, i == 0 ? c0 : z);
          else { sn = mfma32(ka2, q3, sn); if (i < 7) { ka1 = lds128(ka + (i + 1) * 512); ka2 = lds128(kb + (i + 1) * 512); } }
        }
        SB();
#pragma unroll
        for (int r = 4; r < 8; ++r) p[r] = __builtin_amdgcn_exp2f(sc[8 * u + r]);
        const u32x4 pq = {pk(p[0], p[1]), pk(p[2], p[3]), pk(p[4], p[5]), pk(p[6], p[7])};
        if (i < LAG) { ph[i][u] = pq; asm volatile("" : "+v"(ph[i][u])); }
        else o = mfma32(vf[u], pq, o);
        if (i >= LAG && i < 2 * LAG) o = mfma32(vh[u], ph[i - LAG][u], o);
      }
      sc = sn;
    }
    SB();
    float lsum;
    { auto r = __builtin_amdgcn_permlane32_swap(__float_as_uint(o[8]), __float_as_uint(o[8]), false, false); lsum = __uint_as_float(r[1]); }
    const float inv = __builtin_amdgcn_rcpf(lsum);
    const f32x2 chk = ((f32x2{o[0], o[1]} + f32x2{o[2], o[3]}) + (f32x2{o[4], o[5]} + f32x2{o[6], o[7]})) + (f32x2{o[8], o[9]} + f32x2{o[10], o[11]});
    const bool ok = lsum <= 1e15f && lsum >= 0.25f && fabsf(chk.x + chk.y) <= 3e38f;
    ow = widen32(pk(o[0] * inv, o[1] * inv), pk(o[2] * inv, o[3] * inv), pk(o[4] * inv, o[5] * inv), pk(o[6] * inv, o[7] * inv));
    oc = u32x2{pk(o[8] * inv, o[9] * inv), pk(o[10] * inv, o[11] * inv)};
    asm volatile("" : "+v"(ow), "+v"(oc));
    return __all(ok);
  };
  if (__builtin_expect(!softmax_pv(IC<0>{}), 0)) {
    masks();
    k1 = lds128(ka + 4096); k2 = lds128(kb + 4096);
    float m = -3e38f;
#pragma unroll 1
    for (int t = 0; t < 9; ++t) {
      f32x16 sv = mfma32(lds128(ka + t * 512), q1, z);
      sv = mfma32(lds128(kb + t * 512), q2, sv);
#pragma unroll
      for (int r = 0; r < 16; ++r) m = fmaxf(m, sv[r] + (t == 0 ? c0[r] : t == 8 ? c8[r] : 0.f));
    }
    auto r = __builtin_amdgcn_permlane32_swap(__float_as_uint(m), __float_as_uint(m), false, false);
    shift = fmaxf(__uint_as_float(r[0]), __uint_as_float(r[1]));
    softmax_pv(IC<1>{});
  }
  st_dev(ow, rws, v16, OFF_O + (8 * c + w) * GS_O + (head >> 3) * 10240 + (head & 7) * 1024);
  __builtin_amdgcn_raw_buffer_store_b64(oc, rws, vlo >> 1, OFF_O + (8 * c + w) * GS_O + (head >> 3) * 10240 + 8192 + (head & 7) * 256, 16);
  __builtin_amdgcn_s_setprio(2);

  const u32 vq = opaque(g4 * 512 + l15 * 16);
  const u32 s0l = opaque(L_S0 + v16);
  f32x4 csw[3] = {};
  const u32 csa = opaque((8 * hh + q4) * 192 + 32 * blk + 8 * p4);
  auto colsum = [&](u32 s) {
    const u32 a = csa + s;
    u32x4 f[3];
#pragma unroll
    for (int ct = 0; ct < 3; ++ct) f[ct] = ldstr(a + ct * 64, a + ct * 64 + 768);
    asm volatile("" : "+v"(f[0]), "+v"(f[1]), "+v"(f[2])::"memory");
#pragma unroll
    for (int ct = 0; ct < 3; ++ct) { *(LDS u32x4*)(size_t)(s + v16 + ct * 1024) = f[ct]; csw[ct] = mfma16(sel, f[ct], csw[ct]); }
  };
  auto w1done = [&]() {
#pragma unroll
    for (int ct = 0; ct < 3; ++ct) *(LDS float*)(size_t)(L_CSP + w * 384 + ct * 128 + l31 * 4) = csw[ct][0];
    if (lds_ticket(C_W1) == 7) {
#pragma unroll
      for (int ct = 0; ct < 3; ++ct) {
        const u32 a = L_CSP + hh * 1536 + ct * 128 + l31 * 4;
        const float cs = halves_sum((*(LDS float*)(size_t)a + *(LDS float*)(size_t)(a + 384)) + (*(LDS float*)(size_t)(a + 768) + *(LDS float*)(size_t)(a + 1152)));
        const u32 c11 = pk(cs, cs);
        const float c2 = cs - bflo(c11);
        *(LDS u32x4*)(size_t)(L_AUG + ct * 512 + l31 * 16) = u32x4{c11, pk(c2, c2), *(LDS unsigned short*)(size_t)(L_B1 + ct * 64 + l31 * 2) * 0x10001u, 0u};
      }
      lds_inc(C_AUG);
    }
  };
  const u32 pv2 = lane < 8 ? (p4 * 8 + (lane >> 2)) * 1024 : OOB;
  f32x4 acc[2][5] = {};
  u32 pk4 = tag, pk0 = tag;
  {
    const u32 ps2 = wg35(8 * c + w) + (4 * half + (head >> 3)) * 128 + (head & 7) * 4;
    lds_inc(C_FREE);
    VMCNT(4);
    colsum(w1n[2]); colsum(w1n[3]);
    if (half) colsum(w1n[4]);
    asm volatile("" : "+v"(csw[0]), "+v"(csw[1]), "+v"(csw[2]));
    DRAIN();
    post(rws, pv2, ps2, tag);
    if (!half) {
      if (lds_ticket(C_AGG) == 3) post(rws, lane < 32 ? ((lane & 7) * 32 + (lane >> 3) * 2) * 128 : OOB, OFF_MB0 + ((j & 3) * 8 + xo) * 128 + (pr * 8 + (j >> 2)) * 4, tag);
    } else {
      lds_wait(C_FREE, 8);
      w1step(w1n[0]); w1step(w1n[1]);
    }
    if (b == 0 && w == 3) {
      u32 n = 0;
#pragma unroll 1
      for (;;) {
        const u32x4 f = ld_dev(rws, v16, OFF_SEEN);
        asm volatile("" ::: "memory");
        if (__all(f.x == tag && f.y == tag && f.z == tag && f.w == tag) || ++n > 16384) break;
        __builtin_amdgcn_s_sleep(32);
      }
      set_flag(rws, OFF_EPOCH, tag);
    }
#pragma unroll
    for (int m2 = 0; m2 < 2; ++m2) {
      acc[m2][0] = f32x4{bflo(xr[0][m2].x), bfhi(xr[0][m2].x), bflo(xr[0][m2].y), bfhi(xr[0][m2].y)};
      acc[m2][4] = f32x4{bflo(xr[1][m2].x), bfhi(xr[1][m2].x), bflo(xr[1][m2].y), bfhi(xr[1][m2].y)};
      asm volatile("" : "+v"(acc[m2][0]), "+v"(acc[m2][4]));
    }
    const u32 so = sopaque(OFF_O + gr * GS_O + w4 * 10240), vq8 = opaque(g4 * 512 + l15 * 8);
    await(rws, v4, mb, tag, 0xffull);
    u32x4 af[5][2];
#pragma unroll
    for (int m2 = 0; m2 < 2; ++m2) {
#pragma unroll
      for (int si = 0; si < 4; ++si) { af[si][m2] = ld_at(rws, vq, so, si * 2048 + m2 * 256); SB(); }
      const u32x2 lo = __builtin_amdgcn_raw_buffer_load_b64(rws, vq8 + m2 * 128, so + 8192, 16), hi = __builtin_amdgcn_raw_buffer_load_b64(rws, vq8 + m2 * 128 + 256, so + 8192, 16);
      af[4][m2] = u32x4{lo.x, lo.y, hi.x, hi.y};
      SB();
    }
    SB();
    peek(pk0, rws, v4, OFF_MB0 + b * 128, 0xffffffffull);
    if (half) {
      colsum(w1n[0]); colsum(w1n[1]);
      w1done();
    }
    lds_wait(half ? C_SCR : C_AGG, 4);
    u32 xa[4];
#pragma unroll
    for (int cc = 1; cc < 5; ++cc) xa[cc - 1] = opaque(s0l + (cc < 4 ? (((w4 + cc) & 3) * 3 + cc - 1) * 2048 : 24576 + w4 * 2048));
#pragma unroll
    for (int n = 0; n < 10; ++n) {
      SB();
#pragma unroll
      for (int cc = 4; cc >= 0; --cc) acc[n / 5][cc] = mfma16(wf[n % 5][cc], af[n % 5][n / 5], acc[n / 5][cc]);
      if (n >= 5 && n < 9) lds_st4(xa[8 - n], acc[0][9 - n]);
    }
    SB();
#pragma unroll
    for (int cc = 4; cc >= 1; --cc) lds_st4(xa[cc - 1] + 1024, acc[1][cc]);
  }
  lds_inc(C_X3 + half * 4);
  asm volatile("s_waitcnt vmcnt(0)" : "+v"(pk0)::"memory");
  const bool w2ok = __all(pk0 == tag);
  lds_wait(C_X3 + half * 4, 4);
  u32 hp[2][2][2];
  {
    f32x4 t[2][2][3];
    auto rd = [&](int pp) {
      const u32 a = pp ? s0l + 24576 + 2048 : s0l + w4 * 6144;
#pragma unroll
      for (int n = 0; n < 6; ++n) t[pp][n / 3][n % 3] = ldsf4(a + (n % 3) * 2048 + (n / 3) * 1024);
    };
    auto fin = [&](int pp) {
#pragma unroll
      for (int m2 = 0; m2 < 2; ++m2) {
        const f32x4 v = (acc[m2][pp ? 4 : 0] + t[pp][m2][0]) + (t[pp][m2][1] + t[pp][m2][2]);
        hp[pp][m2][0] = pk(v.x, v.y);
        hp[pp][m2][1] = pk(v.z, v.w);
      }
      st_dev(widen16(hp[pp][0][0], hp[pp][0][1], hp[pp][1][0], hp[pp][1][1]), rws, v16, OFF_H + gr * GS_O + (5 * cb3 + (pp ? 4 : w4)) * 1024);
    };
    if (w4 == 0) {
      rd(0); rd(1); SB(); fin(0); fin(1);
    } else {
      rd(0); SB(); fin(0);
      hp[1][0][0] = hp[1][0][1] = hp[1][1][0] = hp[1][1][1] = 0u;
    }
  }
  lds_inc(C_SCR);
  if (__builtin_expect(!w2ok, 0)) await(rws, v4, OFF_MB0 + b * 128, tag, 0xffffffffull);
#pragma unroll
  for (int n = 0; n < W2A; ++n) ld_w2(n);
  {
    float sv[2], qv[2];
#pragma unroll
    for (int m2 = 0; m2 < 2; ++m2) {
      sv[m2] = 0.f; qv[m2] = 0.f;
#pragma unroll
      for (int pp = 0; pp < 2; ++pp)
#pragma unroll
        for (int k = 0; k < 2; ++k) { sv[m2] = dot2(hp[pp][m2][k], 0x3f803f80u, sv[m2]); qv[m2] = dot2(hp[pp][m2][k], hp[pp][m2][k], qv[m2]); }
    }
    auto r1 = __builtin_amdgcn_permlane16_swap(__float_as_uint(sv[0]), __float_as_uint(sv[1]), false, false);
    auto r2 = __builtin_amdgcn_permlane16_swap(__float_as_uint(qv[0]), __float_as_uint(qv[1]), false, false);
    auto r3_ = __builtin_amdgcn_permlane32_swap(__float_as_uint(__uint_as_float(r1[0]) + __uint_as_float(r1[1])),
                                                __float_as_uint(__uint_as_float(r2[0]) + __uint_as_float(r2[1])), false, false);
    *(LDS float*)(size_t)(L_HS + w * 256 + lane * 4) = __uint_as_float(r3_[0]) + __uint_as_float(r3_[1]);
  }
  DRAIN();
  if (lds_ticket(C_HS + half * 4) == 3) {
    post(rws, lane < 16 ? ((lane & 7) * 8 + (lane >> 3)) * 1024 : OOB, OFF_MB + ((gr & 8) * 16 + (gr & 2) * 32 + (gr >> 4) * 2) * 1024 + (4 * half + 2 * (gr & 1) + (cb3 >> 2)) * 128 + (8 + (cb3 & 3)) * 4, tag);
    const u32 a = opaque(L_HS + half * 1024 + lane * 4);
    const float t = (*(LDS float*)(size_t)a + *(LDS float*)(size_t)(a + 256)) + (*(LDS float*)(size_t)(a + 512) + *(LDS float*)(size_t)(a + 768));
    __builtin_amdgcn_raw_buffer_store_b64(u32x2{__float_as_uint(t), tag}, rws, lane * 8, OFF_HS + (gr * 8 + cb3) * 512, 16);
  }
  if (!half) {
    lds_wait(C_FREE, 8);
    w1step(w1n[0]); w1step(w1n[1]); w1step(w1n[4]);
  }
#pragma unroll
  for (int n = W2A; n < W2B; ++n) ld_w2(n);

  {
    u32 so[4], wa[4];
#pragma unroll
    for (int cc = 0; cc < 4; ++cc) {
      so[cc] = sopaque(OFF_H + g4r * GS_O + kh * 20480 + ((cc + cb4) & 3) * 5120);
      wa[cc] = opaque(kh * 61440 + ((cc + cb4) & 3) * 15360 + (lane ^ (kh << 4)) * 16);
    }
    const u32 wx = opaque(L_AUG + (l31 ^ (kh << 4)) * 16);
    if (!half) { VMW2(); colsum(w1n[0]); colsum(w1n[1]); colsum(w1n[4]); w1done(); }
    await(rws, v4, mb + 32, tag, 0xfull);
    u32x4 hf[4][5];
#pragma unroll
    for (int n = 0; n < HF0; ++n) { hf[n / 5][n % 5] = ld_at(rws, v16, so[n / 5], (n % 5) * 1024); SB(); }
    u32x2 hs[8];
    const u32 hso = OFF_HS + g4r * 4096, v8 = opaque(lane * 8);
    auto load_hs = [&]() {
#pragma unroll
      for (int k = 0; k < 8; ++k) hs[k] = __builtin_amdgcn_raw_buffer_load_b64(rws, v8 + k * 512, hso, 16);
    };
    SB();
    lds_wait(C_W1, 8);
    f32x16 a4[3] = {};
    u32x4 ring[6];
    auto w1frag = [&](int n) { return lds128(wa[n / 15] + (n % 15) * 1024); };
#pragma unroll
    for (int n = 0; n < 6; ++n) ring[n] = w1frag(n);
    float rs, s_a, s_b;
    u32 bad, gd = 0;
    u32x4 bx;
    auto stats = [&](int k) {
      if (k == 0) { bad = (hs[0].y ^ tag) | (hs[1].y ^ tag) | (hs[2].y ^ tag) | (hs[3].y ^ tag); asm volatile("" : "+v"(bad)); }
      if (k == 1) { bad |= (hs[4].y ^ tag) | (hs[5].y ^ tag) | (hs[6].y ^ tag) | (hs[7].y ^ tag); asm volatile("" : "+v"(bad)); }
      if (k == 2) {
        s_a = (__uint_as_float(hs[0].x) + __uint_as_float(hs[1].x)) + (__uint_as_float(hs[2].x) + __uint_as_float(hs[3].x));
        asm volatile("" : "+v"(s_a));
      }
      if (k == 3) {
        s_a += (__uint_as_float(hs[4].x) + __uint_as_float(hs[5].x)) + (__uint_as_float(hs[6].x) + __uint_as_float(hs[7].x));
        s_a *= 1.0f / 640.0f;
        asm volatile("" : "+v"(s_a));
      }
      if (k == 4) {
        const auto rt = __builtin_amdgcn_permlane32_swap(__float_as_uint(s_a), __float_as_uint(s_a), false, false);
        s_a = __uint_as_float(rt[0]);
        s_b = fmaxf(__uint_as_float(rt[1]) - s_a * s_a, 0.f);
        asm volatile("" : "+v"(s_a), "+v"(s_b));
      }
      if (k == 5) { const float v1 = s_b + 1e-5f; s_b = v1 * (s_b * __builtin_amdgcn_rcpf(v1) + 1e-5f); asm volatile("" : "+v"(s_b)); }
      if (k == 6) { rs = __builtin_amdgcn_rsqf(s_b); s_a = kh ? s_b * rs : -s_a; asm volatile("" : "+v"(rs), "+v"(s_a)); }
      if (k == 7) { s_b = bflo(pk(s_a, 0.f)); s_a -= s_b; asm volatile("" : "+v"(s_a), "+v"(s_b)); }
      if (k == 8) {
        const u32 tp = hh ? 0u : pk(s_b, s_a);
        bx = u32x4{kh ? 0u : tp, kh ? 0u : tp, kh ? tp : 0u, 0u};
        asm volatile("" : "+v"(bx));
      }
    };
    auto step = [&](int n) __attribute__((always_inline)) {
      SB();
      a4[n % 3] = mfma32(ring[n % 6], hf[n / 15][(n % 15) / 3], a4[n % 3]);
      if (n + 6 < 60) ring[n % 6] = w1frag(n + 6);
      else if (n + 6 < 63) ring[n % 6] = lds128(wx + (n + 6 - 60) * 512);
      if (n == 48) gd = *(volatile LDS u32*)(size_t)C_AUG;
      if (n == 57) gd = *(volatile LDS u32*)(size_t)C_SCR;
      if (n % 3 == 2 && n / 3 + HF0 < 20) hf[(n / 3 + HF0) / 5][(n / 3 + HF0) % 5] = ld_at(rws, v16, so[(n / 3 + HF0) / 5], ((n / 3 + HF0) % 5) * 1024);
      if (n >= 31 && n < 39) hs[n - 31] = __builtin_amdgcn_raw_buffer_load_b64(rws, v8 + (n - 31) * 512, hso, 16);
      if (n >= 42 && n % 3 == 0 && W2B + n / 3 - 14 < 30) ld_w2(W2B + n / 3 - 14);
      if (n >= 51) stats(n - 51);
    };
#pragma unroll
    for (int n = 0; n < 27; ++n) step(n);
#pragma unroll
    for (int n = 27; n < 54; ++n) step(n);
    if (__builtin_expect(__builtin_amdgcn_readfirstlane(gd) == 0, 0)) lds_wait(C_AUG, 1);
#pragma unroll
    for (int n = 54; n < 60; ++n) step(n);
    SB();
    if (__builtin_expect(__any(bad != 0), 0)) {
      u32 it = 0;
#pragma unroll 1
      do {
        load_hs();
        asm volatile("" ::: "memory");
#pragma unroll
        for (int k = 0; k < 9; ++k) stats(k);
      } while (__any(bad != 0) && ++it < 4096);
      if (it >= 4096) give_up(rws);
    }
#pragma unroll
    for (int ct = 0; ct < 3; ++ct) { a4[ct] = mfma32(ring[ct], bx, a4[ct]); SB(); }
    if (__builtin_expect(__builtin_amdgcn_readfirstlane(gd) < 8 + 4 * half, 0)) lds_wait(C_SCR, 8 + 4 * half);
    lds_inc(C_FREE2);
    {
      const u32 a = s0l + w4 * 6144;
#pragma unroll
      for (int ct = 0; ct < 3; ++ct) {
        lds_st4(a + ct * 2048, f32x4{a4[ct][8], a4[ct][9], a4[ct][10], a4[ct][11]});
        lds_st4(a + ct * 2048 + 1024, f32x4{a4[ct][12], a4[ct][13], a4[ct][14], a4[ct][15]});
      }
    }
    lds_inc(C_X4 + (w >> 1) * 4);
#pragma unroll
    for (int n = W2B + 6; n < 30; ++n) ld_w2(n);
    lds_wait(C_X4 + (w >> 1) * 4, 2);
    const u32 oa = s0l + (w4 ^ 1) * 6144;
    f32x4 ot[6];
#pragma unroll
    for (int k = 0; k < 6; ++k) ot[k] = ldsf4(oa + k * 1024);
    SB();
    lds_inc(C_SCR);
    __builtin_amdgcn_s_setprio(0);
#pragma unroll
    for (int ct = 0; ct < 3; ++ct) {
      u32 d[4];
#pragma unroll
      for (int g = 0; g < 4; ++g) {
        const f32x2 u = (f32x2{a4[ct][2 * g], a4[ct][2 * g + 1]} + f32x2{ot[2 * ct + (g >> 1)][(2 * g) & 3], ot[2 * ct + (g >> 1)][(2 * g + 1) & 3]}) * rs;
        const f32x2 z = u * (u * u * -0.1029432397f + -2.302208198f);
        const f32x2 dn = f32x2{__builtin_amdgcn_exp2f(z.x), __builtin_amdgcn_exp2f(z.y)} + 1.0f;
        const f32x2 y = u * f32x2{__builtin_amdgcn_rcpf(dn.x), __builtin_amdgcn_rcpf(dn.y)};
        d[g] = pk(y.x, y.y);
      }
      st_dev(widen32(d[0], d[1], d[2], d[3]), rws, v16, OFF_H1 + g4r * GS_H1 + (6 * cb4 + 2 * ct + kh) * 1024);
    }
    __builtin_amdgcn_s_setprio(2);
    peek(pk4, rws, v4, mb + 48, 15);
    __builtin_amdgcn_s_waitcnt(0x0F70);
    asm volatile("s_waitcnt vmcnt(0)" : "+v"(pk4)::"memory");
    post(rws, pv2, wg35(g4r) + (cb4 >> 1) * 128 + (12 + 4 * half + (cb4 & 1) * 2 + kh) * 4, tag);
  }
  u32x2 b2r[2];
#pragma unroll
  for (int pp = 0; pp < 2; ++pp) b2r[pp] = *(LDS u32x2*)(size_t)(L_B2 + 32 * (pp ? 4 : w4) + 8 * g4);

  const rsrc_t ro = mkrsrc(pout, 2048 * 1280);
  u32x4 af[2][6];
  u32 so5[6];
#pragma unroll
  for (int si = 0; si < 6; ++si) so5[si] = sopaque(OFF_H1 + g3 * GS_H1 + w * 12288 + rstep(si) * 2048);
  auto ld5 = [&](int u, int si) { af[u & 1][si] = ld_at(rws, vq, so5[si] + (u >> 1) * 4 * GS_H1, (u & 1) * 256); };
  f32x4 keep[2] = {};
  const u32 pa0 = opaque(w * 1024 + v16), pa1 = opaque(79872 + w * 1024 + v16);
  auto tile5 = [&](int u, bool ahead) __attribute__((always_inline)) {
    f32x4 a5[5] = {};
#pragma unroll
    for (int si = 0; si < 6; ++si) {
      if (ahead) ld5(u + 1, si);
#pragma unroll
      for (int ct = 0; ct < 5; ++ct) a5[ct] = mfma16(w2f[si][ct], af[u & 1][si], a5[ct]);
      SB();
    }
#pragma unroll
    for (int ct = 0; ct < 4; ++ct) lds_st4((u >> 1 ? pa1 : pa0) + (ct * 2 + (u & 1)) * 8192, a5[ct]);
    if (w == 4 * (u >> 1)) keep[u & 1] = a5[4];
    else lds_st4((u >> 1) * 79872 + 65536 + ((w - 4 * (u >> 1) - 1) & 7) * 2048 + (u & 1) * 1024 + v16, a5[4]);
    SB();
  };
  if (!__all(pk4 == tag)) await(rws, v4, mb + 48, tag, 0xfull);
#pragma unroll
  for (int si = 0; si < 6; ++si) { ld5(0, si); SB(); }
  peek(pk4, rws, v4, mb + 64, 15);
  lds_wait(C_FREE2, 8);
  tile5(0, true);
  asm volatile("s_waitcnt vmcnt(6)" : "+v"(pk4)::"memory");
  if (__all(pk4 == tag)) {
    tile5(1, true);
  } else {
    peek(pk4, rws, v4, mb + 64, 15);
    tile5(1, false);
    asm volatile("s_waitcnt vmcnt(0)" : "+v"(pk4)::"memory");
    if (!__all(pk4 == tag)) await(rws, v4, mb + 64, tag, 0xfull);
#pragma unroll
    for (int si = 0; si < 6; ++si) { ld5(2, si); SB(); }
  }
  lds_wait(C_SCR, 16);
  tile5(2, true);
  tile5(3, false);
  f32x4 base[2][2];
#pragma unroll
  for (int pp = 0; pp < 2; ++pp)
#pragma unroll
    for (int m2 = 0; m2 < 2; ++m2) {
      base[pp][m2] = f32x4{bflo(hp[pp][m2][0]) + bflo(b2r[pp].x), bfhi(hp[pp][m2][0]) + bfhi(b2r[pp].x), bflo(hp[pp][m2][1]) + bflo(b2r[pp].y),
                           bfhi(hp[pp][m2][1]) + bfhi(b2r[pp].y)};
      if (pp) base[pp][m2] += keep[m2];
      asm volatile("" : "+v"(base[pp][m2]));
    }
  const u32 fa = opaque(half * 79872 + w4 * 16384 + v16), vo5 = opaque(l31 * 1280 + hh * 16);
  BAR();
#pragma unroll
  for (int pp = 0; pp < 2; ++pp) {
    if (pp == 0 || w4 == 0) {
      u32 d[2][2];
#pragma unroll
      for (int m2 = 0; m2 < 2; ++m2) {
        f32x4 t[8];
#pragma unroll
        for (int k = 0; k < 8 - pp; ++k) t[k] = pp ? ldsf4(fa + 65536 - 8192 + 8192 + k * 2048 + m2 * 1024) : ldsf4(fa + m2 * 8192 + k * 1024);
        if (pp) t[7] = f32x4{0.f, 0.f, 0.f, 0.f};
        const f32x4 v = base[pp][m2] + (((t[0] + t[1]) + (t[2] + t[3])) + ((t[4] + t[5]) + (pp ? t[6] : t[6] + t[7])));
        d[m2][0] = pk(v.x, v.y);
        d[m2][1] = pk(v.z, v.w);
      }
      st_plain(widen16(d[0][0], d[0][1], d[1][0], d[1][1]), ro, vo5, gr * 40960 + (80 * cb3 + 16 * (pp ? 4 : w4)) * 2);
    }
  }
}

extern "C" void kernel_launch(void* const* d_in, const int* in_sizes, int n_in, void* d_out, int out_size, void* d_ws, size_t ws_size,
                              hipStream_t stream) {
  fused_block<<<256, 512, 0, stream>>>(d_in[0], d_in[1], d_in[2], d_in[4], d_in[5], d_in[6], d_in[7], d_out, d_ws);
}
```
